```python
import torch

def _softmax(x, axis=-1):
    return torch.softmax(x, dim=axis)
def _gelu(x):
    return torch.nn.functional.gelu(x, approximate="tanh")
def _silu(x):
    return torch.nn.functional.silu(x)
def _sigmoid(x):
    return torch.sigmoid(x)
def _layernorm(x, axis=-1, eps=1e-5):
    m = x.mean(dim=axis, keepdim=True)
    v = x.var(dim=axis, keepdim=True, unbiased=False)
    return (x - m) / torch.sqrt(v + eps)
def _scatter_add(tgt, idx, src, axis=0):
    return tgt.clone().index_add_(axis, idx, src)
def _segment_sum(src, seg, n):
    out = torch.zeros((n,) + src.shape[1:], dtype=src.dtype)
    return out.index_add_(0, seg, src)
def _pad(x, pw):
    flat = tuple(v for pair in reversed(pw) for v in pair)
    return torch.nn.functional.pad(x, flat)

kernel_name = 'standalone__mpnn_edge_mlp__graph'

def setup_inputs():

    _w = (lambda a: (lambda t: t.to(torch.bfloat16) if t.is_floating_point() else t)(torch.as_tensor(a)))
    N, deg, D, De = 25000, 32, 64, max(16, 1)
    E = N * deg
    return {
        "x": _w((lambda *s: torch.randn(*s, dtype=torch.bfloat16) * 0.05)(N, D)),
        "e_attr": _w((lambda *s: torch.randn(*s, dtype=torch.bfloat16) * 0.4)(E, De)),
        "src": _w((lambda lo, hi, *s: torch.randint(lo, hi, s))(0, N, E).to(torch.int64)),
        "dst": _w((lambda lo, hi, *s: torch.randint(lo, hi, s))(0, N, E).to(torch.int64)),
        "w_msg": _w((lambda *s: torch.randn(*s, dtype=torch.bfloat16) * 0.4)(2 * D + De, D)),
        "w_upd": _w((lambda *s: torch.randn(*s, dtype=torch.bfloat16) * 0.05)(2 * D, D)),
    }

def reference(x, e_attr, src, dst, w_msg, w_upd):
    N, D = x.shape
    m_in = torch.concatenate([x[src], x[dst], e_attr], axis=-1)
    msg = _silu(m_in @ w_msg)
    agg = _segment_sum(msg, dst, N)
    return _silu(torch.concatenate([x, agg], axis=-1) @ w_upd)
```

```cpp
#include <hip/hip_runtime.h>
#include <hip/hip_bf16.h>
#include <stdint.h>

#define NN 25000
#define EE 800000
#define DD 64
#define DE 16

__device__ __forceinline__ float bf2f(unsigned short v) { return __uint_as_float(((unsigned)v) << 16); }

__global__ void k_zero(float* agg, int n) {
    int i = blockIdx.x * blockDim.x + threadIdx.x;
    if (i < n) agg[i] = 0.f;
}

__global__ __launch_bounds__(256) void k_edge(const unsigned short* x, const unsigned short* ea, const int* src, const int* dst,
                       const unsigned short* wm, float* agg, int E) {
    __shared__ float w[144 * 64];
    for (int i = threadIdx.x; i < 144 * 64; i += blockDim.x) w[i] = bf2f(wm[i]);
    __syncthreads();
    int lane = threadIdx.x & 63;
    int wv = (blockIdx.x * blockDim.x + threadIdx.x) >> 6;
    int nw = (gridDim.x * blockDim.x) >> 6;
    for (int e = wv; e < E; e += nw) {
        int s = src[e], d = dst[e];
        float xs = bf2f(x[s * 64 + lane]);
        float xd = bf2f(x[d * 64 + lane]);
        float ev = lane < 16 ? bf2f(ea[e * 16 + lane]) : 0.f;
        float acc = 0.f;
        _Pragma("unroll 4") for (int k = 0; k < 64; k++) acc += __shfl(xs, k) * w[k * 64 + lane];
        _Pragma("unroll 4") for (int k = 0; k < 64; k++) acc += __shfl(xd, k) * w[(64 + k) * 64 + lane];
        for (int k = 0; k < 16; k++) acc += __shfl(ev, k) * w[(128 + k) * 64 + lane];
        float m = acc / (1.f + __expf(-acc));
        atomicAdd(&agg[d * 64 + lane], m);
    }
}

__global__ __launch_bounds__(256) void k_upd(const unsigned short* x, const float* agg, const unsigned short* wu, __hip_bfloat16* out, int N) {
    __shared__ float w[128 * 64];
    for (int i = threadIdx.x; i < 128 * 64; i += blockDim.x) w[i] = bf2f(wu[i]);
    __syncthreads();
    int lane = threadIdx.x & 63;
    int wv = (blockIdx.x * blockDim.x + threadIdx.x) >> 6;
    int nw = (gridDim.x * blockDim.x) >> 6;
    for (int n = wv; n < N; n += nw) {
        float xs = bf2f(x[n * 64 + lane]);
        float ag = __bfloat162float(__float2bfloat16(agg[n * 64 + lane]));
        float acc = 0.f;
        _Pragma("unroll 4") for (int k = 0; k < 64; k++) acc += __shfl(xs, k) * w[k * 64 + lane];
        _Pragma("unroll 4") for (int k = 0; k < 64; k++) acc += __shfl(ag, k) * w[(64 + k) * 64 + lane];
        float m = acc / (1.f + __expf(-acc));
        out[n * 64 + lane] = __float2bfloat16(m);
    }
}

extern "C" void kernel_launch(void* const* d_in, const int* in_sizes, int n_in,
                              void* d_out, int out_size, void* d_ws, size_t ws_size,
                              hipStream_t stream) {
    const unsigned short* x = (const unsigned short*)d_in[0];
    const unsigned short* ea = (const unsigned short*)d_in[1];
    const int* src = (const int*)d_in[2];
    const int* dst = (const int*)d_in[3];
    const unsigned short* wm = (const unsigned short*)d_in[4];
    const unsigned short* wu = (const unsigned short*)d_in[5];
    float* agg = (float*)d_ws;
    int N = in_sizes[0] / 64, E = in_sizes[2];
    k_zero<<<(N * 64 + 255) / 256, 256, 0, stream>>>(agg, N * 64);
    k_edge<<<2048, 256, 0, stream>>>(x, ea, src, dst, wm, agg, E);
    k_upd<<<1024, 256, 0, stream>>>(x, agg, wu, (__hip_bfloat16*)d_out, N);
}
```

```cpp
#include <hip/hip_runtime.h>
#include <hip/hip_bf16.h>
#include <stdint.h>

#define NN 25000
#define EE 800000
#define DD 64
#define DE 16

__device__ __forceinline__ float bf2f(unsigned short v) { return __uint_as_float(((unsigned)v) << 16); }

__global__ void k_zero(float* agg, int n) {
    int i = blockIdx.x * blockDim.x + threadIdx.x;
    if (i < n) agg[i] = 0.f;
}

__global__ __launch_bounds__(256) void k_edge(const unsigned short* x, const unsigned short* ea, const int* src, const int* dst,
                       const unsigned short* wm, float* agg, int E) {
    __shared__ float w[144 * 64];
    for (int i = threadIdx.x; i < 144 * 64; i += blockDim.x) w[i] = bf2f(wm[i]);
    __syncthreads();
    int lane = threadIdx.x & 63;
    int wv = (blockIdx.x * blockDim.x + threadIdx.x) >> 6;
    int nw = (gridDim.x * blockDim.x) >> 6;
    for (int e = wv; e < E; e += nw) {
        int s = src[e], d = dst[e];
        float xs = bf2f(x[s * 64 + lane]);
        float xd = bf2f(x[d * 64 + lane]);
        float ev = lane < 16 ? bf2f(ea[e * 16 + lane]) : 0.f;
        float acc = 0.f;
        _Pragma("unroll 4") for (int k = 0; k < 64; k++) acc += __shfl(xs, k) * w[k * 64 + lane];
        _Pragma("unroll 4") for (int k = 0; k < 64; k++) acc += __shfl(xd, k) * w[(64 + k) * 64 + lane];
        for (int k = 0; k < 16; k++) acc += __shfl(ev, k) * w[(128 + k) * 64 + lane];
        float m = acc / (1.f + __expf(-acc));
        atomicAdd(&agg[d * 64 + lane], m);
    }
}

__global__ __launch_bounds__(256) void k_upd(const unsigned short* x, const float* agg, const unsigned short* wu, __hip_bfloat16* out, int N) {
    __shared__ float w[128 * 64];
    for (int i = threadIdx.x; i < 128 * 64; i += blockDim.x) w[i] = bf2f(wu[i]);
    __syncthreads();
    int lane = threadIdx.x & 63;
    int wv = (blockIdx.x * blockDim.x + threadIdx.x) >> 6;
    int nw = (gridDim.x * blockDim.x) >> 6;
    for (int n = wv; n < N; n += nw) {
        float xs = bf2f(x[n * 64 + lane]);
        float ag = __bfloat162float(__float2bfloat16(agg[n * 64 + lane]));
        float acc = 0.f;
        _Pragma("unroll 4") for (int k = 0; k < 64; k++) acc += __shfl(xs, k) * w[k * 64 + lane];
        _Pragma("unroll 4") for (int k = 0; k < 64; k++) acc += __shfl(ag, k) * w[(64 + k) * 64 + lane];
        float m = acc / (1.f + __expf(-acc));
        out[n * 64 + lane] = __float2bfloat16(m);
    }
}

#define NE 0
#define USEGAP 0

typedef __attribute__((ext_vector_type(4))) unsigned u4;
template<int LAUX, int SAUX, int DOSTORE>
__global__ __launch_bounds__(1024) void pa(const char* __restrict__ ea, const char* __restrict__ id0, const char* __restrict__ id1, char* ws, unsigned* out, unsigned long long* ts) {
    unsigned long long t0 = __builtin_amdgcn_s_memrealtime();
    __amdgpu_buffer_rsrc_t re = __builtin_amdgcn_make_buffer_rsrc((void*)(ea + (size_t)blockIdx.x*100000), 0, 100000, 0x00020000);
    __amdgpu_buffer_rsrc_t r0 = __builtin_amdgcn_make_buffer_rsrc((void*)(id0 + (size_t)blockIdx.x*12496), 0, 12496, 0x00020000);
    __amdgpu_buffer_rsrc_t r1 = __builtin_amdgcn_make_buffer_rsrc((void*)(id1 + (size_t)blockIdx.x*12496), 0, 12496, 0x00020000);
    __amdgpu_buffer_rsrc_t rw = __builtin_amdgcn_make_buffer_rsrc((void*)(ws + (size_t)blockIdx.x*112640), 0, 112512, 0x00020000);
    u4 v[9];
    v[7] = __builtin_amdgcn_raw_buffer_load_b128(r0, threadIdx.x*16, 0, LAUX);
    v[8] = __builtin_amdgcn_raw_buffer_load_b128(r1, threadIdx.x*16, 0, LAUX);
#pragma unroll
    for (int i=0;i<7;i++) v[i] = NE ? __builtin_amdgcn_raw_buffer_load_b128(re, threadIdx.x*16 + i*16384, 0, LAUX) : u4{1,2,3,4};
    v[6] += v[7] + v[8];
    asm volatile("s_waitcnt vmcnt(0)" ::: "memory");
    __syncthreads();
    if (DOSTORE) {
#pragma unroll
        for (int i=0;i<7;i++) __builtin_amdgcn_raw_buffer_store_b128(v[i], rw, threadIdx.x*16 + i*16384, 0, SAUX);
    } else {
        u4 a = v[0]; for (int i=1;i<7;i++) a += v[i];
        if (a[0]+a[1]+a[2]+a[3] == 0x12345) out[threadIdx.x] = 1;
    }
    asm volatile("s_waitcnt vmcnt(0)" ::: "memory");
    __syncthreads();
    unsigned long long t2 = __builtin_amdgcn_s_memrealtime();
    if (threadIdx.x == 0) { ts[blockIdx.x*2] = t0; ts[blockIdx.x*2+1] = t2; }
}
__global__ __launch_bounds__(256) void pspin(unsigned long long* ts) {
    unsigned long long tb = __builtin_amdgcn_s_memrealtime();
    __shared__ unsigned long long mn[256], mx[256];
    mn[threadIdx.x] = ts[threadIdx.x*2]; mx[threadIdx.x] = ts[threadIdx.x*2+1];
    __syncthreads();
    unsigned long long a=~0ull,b=0;
    for (int i=0;i<256;i++){ a = mn[i]<a?mn[i]:a; b = mx[i]>b?mx[i]:b; }
    unsigned long long d = USEGAP ? (tb - a) : (b - a);
    if (d > 5000) d = 5000;
    unsigned long long t0 = __builtin_amdgcn_s_memrealtime();
    while (__builtin_amdgcn_s_memrealtime() - t0 < d * 200) { __builtin_amdgcn_s_sleep(8); }
}
extern "C" void kernel_launch(void* const* d_in, const int* in_sizes, int n_in,
                              void* d_out, int out_size, void* d_ws, size_t ws_size,
                              hipStream_t stream) {
    const unsigned short* x = (const unsigned short*)d_in[0];
    const unsigned short* ea = (const unsigned short*)d_in[1];
    const int* src = (const int*)d_in[2];
    const int* dst = (const int*)d_in[3];
    const unsigned short* wm = (const unsigned short*)d_in[4];
    const unsigned short* wu = (const unsigned short*)d_in[5];
    float* agg = (float*)d_ws;
    int N = in_sizes[0] / 64, E = in_sizes[2];

    {
        char* wsb = (char*)d_ws;
        char* dstp = wsb + 8*1024*1024;
        unsigned long long* ts = (unsigned long long*)(wsb + 64*1024*1024);
        pa<0,0,0><<<256,1024,0,stream>>>((const char*)ea,(const char*)src,(const char*)dst,dstp,(unsigned*)(wsb+65*1024*1024),ts);
        pspin<<<1,256,0,stream>>>(ts);
    }
    k_zero<<<(N * 64 + 255) / 256, 256, 0, stream>>>(agg, N * 64);
    k_edge<<<2048, 256, 0, stream>>>(x, ea, src, dst, wm, agg, E);
    k_upd<<<1024, 256, 0, stream>>>(x, agg, wu, (__hip_bfloat16*)d_out, N);
}
```

```cpp
#define SAUX 0
#include <hip/hip_runtime.h>
#include <hip/hip_bf16.h>
#include <stdint.h>

typedef __attribute__((ext_vector_type(4))) unsigned u32x4;
typedef __attribute__((ext_vector_type(3))) unsigned u32x3;
typedef __attribute__((ext_vector_type(2))) unsigned u32x2;
typedef __attribute__((ext_vector_type(8))) short bf16x8;
typedef __attribute__((ext_vector_type(4))) short s16x4;
typedef __attribute__((ext_vector_type(16))) float f32x16;
typedef __attribute__((ext_vector_type(4))) float f32x4;
typedef __attribute__((ext_vector_type(2))) float f32x2;
typedef __attribute__((ext_vector_type(2))) __bf16 bf16x2;
#define LDS __attribute__((address_space(3)))
#define DI __device__ __forceinline__
#define MFMA(a, b, c) __builtin_amdgcn_mfma_f32_32x32x16_bf16((a), (b), (c), 0, 0, 0)

constexpr int NN = 25000, EE = 800000;
constexpr int CH = 3136;
constexpr int NCH = 256;
constexpr int NG = 1024;
constexpr int OFFROW = 1088;
#define C0 256
constexpr size_t WS_E = 0;
constexpr size_t WS_ID = WS_E + (size_t)NCH * CH * 32;
constexpr size_t WS_OFF = WS_ID + (size_t)NCH * CH * 4;
constexpr size_t WS_W = WS_OFF + (size_t)NCH * OFFROW * 2;
constexpr size_t WS_TS = WS_W + 34 * 1024;

DI u32x4 mkrsrc(const void* p, unsigned bytes) {
    uint64_t a = (uint64_t)p;
    u32x4 r;
    r[0] = __builtin_amdgcn_readfirstlane((unsigned)a);
    r[1] = __builtin_amdgcn_readfirstlane((unsigned)(a >> 32) & 0xffffu);
    r[2] = __builtin_amdgcn_readfirstlane(bytes);
    r[3] = 0x00020000u;
    return r;
}
DI void dma16(u32x4 rsrc, unsigned lds, unsigned voff) {
    asm volatile("s_mov_b32 m0, %0\n\ts_nop 0\n\tbuffer_load_dwordx4 %1, %2, 0 offen lds" ::"s"(lds), "v"(voff), "s"(rsrc) : "memory");
}
DI void lds_barrier() { asm volatile("s_waitcnt lgkmcnt(0)\n\ts_barrier" ::: "memory"); }
#define WAITVM(n) asm volatile("s_waitcnt vmcnt(" #n ")" ::: "memory")
DI unsigned ldsaddr(const void* p) { return (unsigned)(size_t)(LDS const char*)p; }
template <class T> DI T ldsr(unsigned a) { return *(LDS const T*)(size_t)a; }
template <class T> DI void ldsw(unsigned a, T v) { *(LDS T*)(size_t)a = v; }
DI float bf2f(unsigned short v) { return __uint_as_float((unsigned)v << 16); }
DI unsigned pk2(float a, float b) { f32x2 v = {a, b}; return __builtin_bit_cast(unsigned, __builtin_convertvector(v, bf16x2)); }
DI unsigned mad16lo(unsigned e, unsigned m, unsigned c) { unsigned r; asm("v_mad_u32_u16 %0, %1, %2, %3" : "=v"(r) : "v"(e), "s"(m), "v"(c)); return r; }
DI unsigned mad16hi(unsigned e, unsigned m, unsigned c) { unsigned r; asm("v_mad_u32_u16 %0, %1, %2, %3 op_sel:[1,0,0,0]" : "=v"(r) : "v"(e), "s"(m), "v"(c)); return r; }
DI int gstart(int g) { return (g * 3125) >> 7; }

#define STAMP(k, i)

template <int CTRL> DI unsigned dpp0(unsigned v) { return __builtin_amdgcn_update_dpp(0u, v, CTRL, 0xf, 0xf, false); }
template <int CTRL, int RM> DI unsigned dppm(unsigned v) { return __builtin_amdgcn_update_dpp(0u, v, CTRL, RM, 0xf, false); }
DI unsigned wave_incl_scan(unsigned v) {
    v += dpp0<0x111>(v); v += dpp0<0x112>(v); v += dpp0<0x114>(v); v += dpp0<0x118>(v);
    v += dppm<0x142, 0xa>(v); v += dppm<0x143, 0xc>(v);
    return v;
}
constexpr int K1_E = 0, K1_SID = CH * 32, K1_INV = K1_SID + CH * 4, K1_HIST = K1_INV + CH * 2, K1_WS = K1_HIST + 4096, K1_LDS = K1_WS + 64;

__global__ __launch_bounds__(1024) void k1(const int* __restrict__ src, const int* __restrict__ dst, const char* __restrict__ ea,
                                           const unsigned short* __restrict__ wm, const unsigned short* __restrict__ wu, char* __restrict__ ws) {
    __shared__ __attribute__((aligned(16))) char lds[K1_LDS];
    STAMP(0, 0);
    const unsigned L = ldsaddr(lds);
    const int tid = threadIdx.x, lane = tid & 63, wv = __builtin_amdgcn_readfirstlane(tid >> 6);
    const int c = blockIdx.x, base = c * CH;
    const int n = min(CH, EE - base);
    const bool hasid = tid * 4 < n;
    u32x4 d4 = {0, 0, 0, 0}, s4 = {0, 0, 0, 0};
    if (hasid) {
        d4 = __builtin_nontemporal_load((const u32x4*)(dst + base) + tid);
        s4 = __builtin_nontemporal_load((const u32x4*)(src + base) + tid);
    }
    ldsw<unsigned>(L + K1_HIST + tid * 4, 0u);
    lds_barrier();
    if (wv >= 13) {
        const u32x4 re = mkrsrc(ea + (size_t)base * 32, n * 32);
        for (int i = wv - 13; i < CH / 32; i += 3) dma16(re, L + K1_E + i * 1024, i * 1024 + lane * 16);
    }
    unsigned g[4], rk[4];
    if (hasid) {
#pragma unroll
        for (int j = 0; j < 4; j++) {
            g[j] = (d4[j] * 128u + 127u) / 3125u;
            rk[j] = __hip_atomic_fetch_add((LDS unsigned*)(size_t)(L + K1_HIST + g[j] * 4), 1u, __ATOMIC_RELAXED, __HIP_MEMORY_SCOPE_WORKGROUP);
        }
    }
    STAMP(0, 1);
    lds_barrier();
    const unsigned cnt = ldsr<unsigned>(L + K1_HIST + tid * 4);
    const unsigned inc = wave_incl_scan(cnt);
    if (lane == 63) ldsw<unsigned>(L + K1_WS + wv * 4, inc);
    lds_barrier();
    unsigned wsum = ldsr<unsigned>(L + K1_WS + (lane & 15) * 4);
    wsum = (lane & 15) < wv ? wsum : 0u;
    wsum += dpp0<0x128>(wsum); wsum += dpp0<0x124>(wsum); wsum += dpp0<0x122>(wsum); wsum += dpp0<0x121>(wsum);
    const unsigned exc = wsum + inc - cnt;
    ldsw<unsigned>(L + K1_HIST + tid * 4, exc);
    unsigned short* orow = (unsigned short*)(ws + WS_OFF) + (size_t)c * OFFROW;
    orow[tid] = (unsigned short)exc;
    if (tid < 4) orow[NG + tid] = (unsigned short)n;
    lds_barrier();
    if (hasid) {
#pragma unroll
        for (int j = 0; j < 4; j++) {
            const unsigned pos = ldsr<unsigned>(L + K1_HIST + g[j] * 4) + rk[j];
            const unsigned dl = d4[j] - ((g[j] * 3125u) >> 7);
            ldsw<unsigned>(L + K1_SID + pos * 4, s4[j] | (dl << 16));
            ldsw<unsigned short>(L + K1_INV + pos * 2, (unsigned short)(tid * 4 + j));
        }
    }
    lds_barrier();
    STAMP(0, 2);
    if (hasid) ((u32x4*)(ws + WS_ID + (size_t)base * 4))[tid] = ldsr<u32x4>(L + K1_SID + tid * 16);
    if (c < 34 && wv == 12) {
        const int col = lane & 31, h = lane >> 5;
        unsigned short v[8];
        float sc = -1.4426950408889634f;
#pragma unroll
        for (int j = 0; j < 8; j++) {
            int k; const unsigned short* w;
            if (c < 8) { k = 16 * (c >> 1) + 8 * h + j; w = wm + k * 64 + (c & 1) * 32 + col; }
            else if (c < 10) { k = 128 + 8 * h + j; w = wm + k * 64 + (c & 1) * 32 + col; }
            else if (c < 18) { k = 64 + 16 * ((c - 10) >> 1) + 8 * h + j; w = wm + k * 64 + (c & 1) * 32 + col; }
            else {
                const int f = c - 18, s = f & 7;
                k = s < 4 ? 16 * s + 8 * h + j : 64 + 16 * (s - 4) + 8 * (j >> 2) + 4 * h + (j & 3);
                w = wu + k * 64 + (f >> 3) * 32 + col;
            }
            v[j] = *w;
        }
        u32x4 o;
        if (c < 10) {
#pragma unroll
            for (int j = 0; j < 4; j++) o[j] = pk2(bf2f(v[2 * j]) * sc, bf2f(v[2 * j + 1]) * sc);
        } else {
#pragma unroll
            for (int j = 0; j < 4; j++) o[j] = v[2 * j] | ((unsigned)v[2 * j + 1] << 16);
        }
        ((u32x4*)(ws + WS_W + c * 1024))[lane] = o;
    }
    unsigned ra[7];
#pragma unroll
    for (int k = 0; k < 7; k++) {
        const int p = min(tid + k * 1024, 2 * n - 1);
        ra[k] = L + K1_E + ldsr<unsigned short>(L + K1_INV + (p >> 1) * 2) * 32 + (p & 1) * 16;
    }
    if (wv >= 13) WAITVM(0);
    lds_barrier();
    STAMP(0, 3);
    const __amdgpu_buffer_rsrc_t ro = __builtin_amdgcn_make_buffer_rsrc(ws + WS_E + (size_t)base * 32, 0, n * 32, 0x00020000);
#pragma unroll
    for (int k = 0; k < 7; k++) {
        if (k < 6 || wv < 2) __builtin_amdgcn_raw_buffer_store_b128(ldsr<u32x4>(ra[k]), ro, (tid + k * 1024) * 16, 0, SAUX);
    }
    STAMP(0, 4);
}

#define XLOOP_ASM \
  "ds_read_b32 v180, v205\n" \
  "ds_read_b128 v[186:189], v207 offset:512\n" \
  "ds_read_b32 v181, v208 offset:512\n" \
  "v_mov_b32_e32 v156, 0\n" \
  "v_mov_b32_e32 v157, 0\n" \
  "v_mov_b32_e32 v158, 0\n" \
  "v_mov_b32_e32 v159, 0\n" \
  "v_mov_b32_e32 v160, 0\n" \
  "v_mov_b32_e32 v161, 0\n" \
  "v_mov_b32_e32 v162, 0\n" \
  "v_mov_b32_e32 v163, 0\n" \
  "v_mov_b32_e32 v164, 0\n" \
  "v_mov_b32_e32 v165, 0\n" \
  "v_mov_b32_e32 v166, 0\n" \
  "v_mov_b32_e32 v167, 0\n" \
  "v_mov_b32_e32 v168, 0\n" \
  "v_mov_b32_e32 v169, 0\n" \
  "v_mov_b32_e32 v170, 0\n" \
  "v_mov_b32_e32 v171, 0\n" \
  "v_mov_b32_e32 v148, 0\n" \
  "v_mov_b32_e32 v149, 0\n" \
  "v_mov_b32_e32 v150, 0\n" \
  "v_mov_b32_e32 v151, 0\n" \
  "v_mov_b32_e32 v152, 0\n" \
  "v_mov_b32_e32 v153, 0\n" \
  "v_mov_b32_e32 v154, 0\n" \
  "v_mov_b32_e32 v155, 0\n" \
  "s_waitcnt lgkmcnt(0)\n" \
  "v_mad_u32_u16 v190, v180, %[c80], v201 op_sel:[1,0,0,0]\n" \
  "ds_read_b128 v[140:143], v190\n" \
  "ds_read_b128 v[144:147], v190 offset:32\n" \
  "ds_read_b32 v180, v205 offset:256\n" \
  "ds_read_b128 v[182:185], v206\n" \
  "s_waitcnt vmcnt(5)\n" \
  "s_cmp_gt_i32 %[rem], 1\n" \
  "s_cbranch_scc1 X1_%=\n" \
  "s_waitcnt vmcnt(0)\n" \
  "X1_%=:\n" \
  "ds_read_b128 v[136:139], v200\n" \
  "ds_read_b128 v[120:123], v196\n" \
  "ds_read_b128 v[124:127], v197\n" \
  "ds_read_b128 v[128:131], v198\n" \
  "ds_read_b128 v[132:135], v199\n" \
  "s_waitcnt lgkmcnt(0)\n" \
  "v_mfma_f32_32x32x16_bf16 v[88:103], v[140:143], v[40:43], 0\n" \
  "s_cmp_gt_i32 %[rem], 2\n" \
  "s_cbranch_scc0 X2_%=\n" \
  "s_add_u32 m0, %[stg], 4096\n" \
  "v_add_u32 v190, v181, v209\n" \
  "buffer_load_dwordx4 v190, %[re], 0 offen lds\n" \
  "s_add_u32 m0, %[stg], 0\n" \
  "v_mad_u32_u16 v191, v186, %[c128], v203\n" \
  "buffer_load_dwordx4 v191, %[rx], 0 offen lds\n" \
  "s_add_u32 m0, %[stg], 1024\n" \
  "v_mad_u32_u16 v192, v187, %[c128], v204\n" \
  "buffer_load_dwordx4 v192, %[rx], 0 offen lds\n" \
  "s_add_u32 m0, %[stg], 2048\n" \
  "v_mad_u32_u16 v193, v188, %[c128], v203\n" \
  "buffer_load_dwordx4 v193, %[rx], 0 offen lds\n" \
  "s_add_u32 m0, %[stg], 3072\n" \
  "v_mad_u32_u16 v194, v189, %[c128], v204\n" \
  "buffer_load_dwordx4 v194, %[rx], 0 offen lds\n" \
  "X2_%=:\n" \
  "v_mfma_f32_32x32x16_bf16 v[88:103], v[144:147], v[48:51], v[88:103]\n" \
  "v_mfma_f32_32x32x16_bf16 v[88:103], v[136:139], v[32:35], v[88:103]\n" \
  "v_mfma_f32_32x32x16_bf16 v[88:103], v[120:123], v[0:3], v[88:103]\n" \
  "v_mfma_f32_32x32x16_bf16 v[88:103], v[124:127], v[8:11], v[88:103]\n" \
  "v_mfma_f32_32x32x16_bf16 v[88:103], v[128:131], v[16:19], v[88:103]\n" \
  "v_mfma_f32_32x32x16_bf16 v[88:103], v[132:135], v[24:27], v[88:103]\n" \
  "LOOP_%=:\n" \
  "s_setprio 1\n" \
  "s_waitcnt lgkmcnt(0)\n" \
  "v_mfma_f32_32x32x16_bf16 v[104:119], v[140:143], v[44:47], 0\n" \
  "s_waitcnt vmcnt(5)\n" \
  "v_cvt_pk_bf16_f32 v172, v156, v157\n" \
  "v_cvt_pk_bf16_f32 v173, v158, v159\n" \
  "s_cmp_gt_i32 %[rem], 2\n" \
  "s_cbranch_scc1 X3_%=\n" \
  "s_waitcnt vmcnt(0)\n" \
  "X3_%=:\n" \
  "v_cvt_pk_bf16_f32 v174, v160, v161\n" \
  "v_cvt_pk_bf16_f32 v175, v162, v163\n" \
  "v_cvt_pk_bf16_f32 v176, v164, v165\n" \
  "v_cvt_pk_bf16_f32 v177, v166, v167\n" \
  "v_cvt_pk_bf16_f32 v178, v168, v169\n" \
  "v_cvt_pk_bf16_f32 v179, v170, v171\n" \
  "v_mad_u32_u16 v190, v180, %[c80], v201 op_sel:[1,0,0,0]\n" \
  "v_mfma_f32_32x32x16_bf16 v[104:119], v[144:147], v[52:55], v[104:119]\n" \
  "ds_read_b128 v[140:143], v190\n" \
  "ds_read_b128 v[144:147], v190 offset:32\n" \
  "v_exp_f32_e32 v156, v88\n" \
  "v_exp_f32_e32 v157, v89\n" \
  "v_exp_f32_e32 v158, v90\n" \
  "v_mfma_f32_32x32x16_bf16 v[72:87], v[172:175], v[148:151], v[72:87]\n" \
  "v_mad_u32_u16 v191, v182, %[c80], v202 op_sel:[1,0,0,0]\n" \
  "v_mad_u32_u16 v192, v183, %[c80], v202 op_sel:[1,0,0,0]\n" \
  "ds_read_b64_tr_b16 v[148:149], v191\n" \
  "ds_read_b64_tr_b16 v[150:151], v192\n" \
  "v_exp_f32_e32 v159, v91\n" \
  "v_exp_f32_e32 v160, v92\n" \
  "v_mfma_f32_32x32x16_bf16 v[72:87], v[176:179], v[152:155], v[72:87]\n" \
  "v_mad_u32_u16 v193, v184, %[c80], v202 op_sel:[1,0,0,0]\n" \
  "v_mad_u32_u16 v194, v185, %[c80], v202 op_sel:[1,0,0,0]\n" \
  "ds_read_b64_tr_b16 v[152:153], v193\n" \
  "ds_read_b64_tr_b16 v[154:155], v194\n" \
  "v_exp_f32_e32 v161, v93\n" \
  "v_exp_f32_e32 v162, v94\n" \
  "v_mfma_f32_32x32x16_bf16 v[104:119], v[136:139], v[36:39], v[104:119]\n" \
  "ds_read_b128 v[136:139], v200 offset:5120\n" \
  "ds_read_b128 v[186:189], v207 offset:768\n" \
  "ds_read_b32 v181, v208 offset:768\n" \
  "v_exp_f32_e32 v163, v95\n" \
  "v_exp_f32_e32 v164, v96\n" \
  "v_exp_f32_e32 v165, v97\n" \
  "v_exp_f32_e32 v166, v98\n" \
  "v_exp_f32_e32 v167, v99\n" \
  "v_exp_f32_e32 v168, v100\n" \
  "v_exp_f32_e32 v169, v101\n" \
  "v_exp_f32_e32 v170, v102\n" \
  "v_exp_f32_e32 v171, v103\n" \
  "s_setprio 0\n" \
  "v_add_f32_e32 v156, 1.0, v156\n" \
  "v_add_f32_e32 v157, 1.0, v157\n" \
  "v_add_f32_e32 v158, 1.0, v158\n" \
  "v_add_f32_e32 v159, 1.0, v159\n" \
  "v_add_f32_e32 v160, 1.0, v160\n" \
  "v_add_f32_e32 v161, 1.0, v161\n" \
  "v_add_f32_e32 v162, 1.0, v162\n" \
  "v_add_f32_e32 v163, 1.0, v163\n" \
  "v_add_f32_e32 v164, 1.0, v164\n" \
  "v_add_f32_e32 v165, 1.0, v165\n" \
  "v_add_f32_e32 v166, 1.0, v166\n" \
  "v_add_f32_e32 v167, 1.0, v167\n" \
  "v_add_f32_e32 v168, 1.0, v168\n" \
  "v_add_f32_e32 v169, 1.0, v169\n" \
  "v_add_f32_e32 v170, 1.0, v170\n" \
  "v_add_f32_e32 v171, 1.0, v171\n" \
  "s_setprio 1\n" \
  "v_mfma_f32_32x32x16_bf16 v[104:119], v[120:123], v[4:7], v[104:119]\n" \
  "ds_read_b128 v[120:123], v196 offset:5120\n" \
  "v_rcp_f32_e32 v156, v156\n" \
  "v_rcp_f32_e32 v157, v157\n" \
  "v_rcp_f32_e32 v158, v158\n" \
  "v_mfma_f32_32x32x16_bf16 v[104:119], v[124:127], v[12:15], v[104:119]\n" \
  "ds_read_b128 v[124:127], v197 offset:5120\n" \
  "v_rcp_f32_e32 v159, v159\n" \
  "v_rcp_f32_e32 v160, v160\n" \
  "v_rcp_f32_e32 v161, v161\n" \
  "v_mfma_f32_32x32x16_bf16 v[104:119], v[128:131], v[20:23], v[104:119]\n" \
  "ds_read_b128 v[128:131], v198 offset:5120\n" \
  "v_rcp_f32_e32 v162, v162\n" \
  "v_rcp_f32_e32 v163, v163\n" \
  "v_rcp_f32_e32 v164, v164\n" \
  "v_mfma_f32_32x32x16_bf16 v[104:119], v[132:135], v[28:31], v[104:119]\n" \
  "ds_read_b128 v[132:135], v199 offset:5120\n" \
  "v_rcp_f32_e32 v165, v165\n" \
  "v_rcp_f32_e32 v166, v166\n" \
  "v_rcp_f32_e32 v167, v167\n" \
  "v_rcp_f32_e32 v168, v168\n" \
  "v_rcp_f32_e32 v169, v169\n" \
  "v_rcp_f32_e32 v170, v170\n" \
  "v_rcp_f32_e32 v171, v171\n" \
  "s_setprio 0\n" \
  "v_mul_f32_e32 v156, v88, v156\n" \
  "v_mul_f32_e32 v157, v89, v157\n" \
  "v_mul_f32_e32 v158, v90, v158\n" \
  "v_mul_f32_e32 v159, v91, v159\n" \
  "v_mul_f32_e32 v160, v92, v160\n" \
  "v_mul_f32_e32 v161, v93, v161\n" \
  "v_mul_f32_e32 v162, v94, v162\n" \
  "v_mul_f32_e32 v163, v95, v163\n" \
  "v_mul_f32_e32 v164, v96, v164\n" \
  "v_mul_f32_e32 v165, v97, v165\n" \
  "v_mul_f32_e32 v166, v98, v166\n" \
  "v_mul_f32_e32 v167, v99, v167\n" \
  "v_mul_f32_e32 v168, v100, v168\n" \
  "v_mul_f32_e32 v169, v101, v169\n" \
  "v_mul_f32_e32 v170, v102, v170\n" \
  "v_mul_f32_e32 v171, v103, v171\n" \
  "s_setprio 1\n" \
  "s_waitcnt lgkmcnt(0)\n" \
  "v_mfma_f32_32x32x16_bf16 v[88:103], v[140:143], v[40:43], 0\n" \
  "v_cvt_pk_bf16_f32 v172, v156, v157\n" \
  "v_cvt_pk_bf16_f32 v173, v158, v159\n" \
  "v_cvt_pk_bf16_f32 v174, v160, v161\n" \
  "v_cvt_pk_bf16_f32 v175, v162, v163\n" \
  "v_cvt_pk_bf16_f32 v176, v164, v165\n" \
  "v_cvt_pk_bf16_f32 v177, v166, v167\n" \
  "v_cvt_pk_bf16_f32 v178, v168, v169\n" \
  "v_cvt_pk_bf16_f32 v179, v170, v171\n" \
  "v_mfma_f32_32x32x16_bf16 v[88:103], v[144:147], v[48:51], v[88:103]\n" \
  "ds_read_b32 v180, v205 offset:512\n" \
  "ds_read_b128 v[182:185], v206 offset:256\n" \
  "v_exp_f32_e32 v156, v104\n" \
  "v_exp_f32_e32 v157, v105\n" \
  "v_exp_f32_e32 v158, v106\n" \
  "v_mfma_f32_32x32x16_bf16 v[56:71], v[172:175], v[148:151], v[56:71]\n" \
  "v_exp_f32_e32 v159, v107\n" \
  "v_exp_f32_e32 v160, v108\n" \
  "v_exp_f32_e32 v161, v109\n" \
  "v_mfma_f32_32x32x16_bf16 v[56:71], v[176:179], v[152:155], v[56:71]\n" \
  "v_exp_f32_e32 v162, v110\n" \
  "v_exp_f32_e32 v163, v111\n" \
  "v_exp_f32_e32 v164, v112\n" \
  "v_mfma_f32_32x32x16_bf16 v[88:103], v[136:139], v[32:35], v[88:103]\n" \
  "s_cmp_gt_i32 %[rem], 3\n" \
  "s_cbranch_scc0 X4_%=\n" \
  "s_add_u32 m0, %[stg], 9216\n" \
  "v_add_u32 v190, v181, v209\n" \
  "buffer_load_dwordx4 v190, %[re], 0 offen lds\n" \
  "s_add_u32 m0, %[stg], 5120\n" \
  "v_mad_u32_u16 v191, v186, %[c128], v203\n" \
  "buffer_load_dwordx4 v191, %[rx], 0 offen lds\n" \
  "s_add_u32 m0, %[stg], 6144\n" \
  "v_mad_u32_u16 v192, v187, %[c128], v204\n" \
  "buffer_load_dwordx4 v192, %[rx], 0 offen lds\n" \
  "s_add_u32 m0, %[stg], 7168\n" \
  "v_mad_u32_u16 v193, v188, %[c128], v203\n" \
  "buffer_load_dwordx4 v193, %[rx], 0 offen lds\n" \
  "s_add_u32 m0, %[stg], 8192\n" \
  "v_mad_u32_u16 v194, v189, %[c128], v204\n" \
  "buffer_load_dwordx4 v194, %[rx], 0 offen lds\n" \
  "X4_%=:\n" \
  "v_exp_f32_e32 v165, v113\n" \
  "v_exp_f32_e32 v166, v114\n" \
  "v_exp_f32_e32 v167, v115\n" \
  "v_exp_f32_e32 v168, v116\n" \
  "v_exp_f32_e32 v169, v117\n" \
  "v_exp_f32_e32 v170, v118\n" \
  "v_exp_f32_e32 v171, v119\n" \
  "s_setprio 0\n" \
  "v_add_f32_e32 v156, 1.0, v156\n" \
  "v_add_f32_e32 v157, 1.0, v157\n" \
  "v_add_f32_e32 v158, 1.0, v158\n" \
  "v_add_f32_e32 v159, 1.0, v159\n" \
  "v_add_f32_e32 v160, 1.0, v160\n" \
  "v_add_f32_e32 v161, 1.0, v161\n" \
  "v_add_f32_e32 v162, 1.0, v162\n" \
  "v_add_f32_e32 v163, 1.0, v163\n" \
  "v_add_f32_e32 v164, 1.0, v164\n" \
  "v_add_f32_e32 v165, 1.0, v165\n" \
  "v_add_f32_e32 v166, 1.0, v166\n" \
  "v_add_f32_e32 v167, 1.0, v167\n" \
  "v_add_f32_e32 v168, 1.0, v168\n" \
  "v_add_f32_e32 v169, 1.0, v169\n" \
  "v_add_f32_e32 v170, 1.0, v170\n" \
  "v_add_f32_e32 v171, 1.0, v171\n" \
  "s_setprio 1\n" \
  "v_mfma_f32_32x32x16_bf16 v[88:103], v[120:123], v[0:3], v[88:103]\n" \
  "v_rcp_f32_e32 v156, v156\n" \
  "v_rcp_f32_e32 v157, v157\n" \
  "v_rcp_f32_e32 v158, v158\n" \
  "v_mfma_f32_32x32x16_bf16 v[88:103], v[124:127], v[8:11], v[88:103]\n" \
  "v_rcp_f32_e32 v159, v159\n" \
  "v_rcp_f32_e32 v160, v160\n" \
  "v_rcp_f32_e32 v161, v161\n" \
  "v_mfma_f32_32x32x16_bf16 v[88:103], v[128:131], v[16:19], v[88:103]\n" \
  "v_rcp_f32_e32 v162, v162\n" \
  "v_rcp_f32_e32 v163, v163\n" \
  "v_rcp_f32_e32 v164, v164\n" \
  "v_mfma_f32_32x32x16_bf16 v[88:103], v[132:135], v[24:27], v[88:103]\n" \
  "v_rcp_f32_e32 v165, v165\n" \
  "v_rcp_f32_e32 v166, v166\n" \
  "v_rcp_f32_e32 v167, v167\n" \
  "v_rcp_f32_e32 v168, v168\n" \
  "v_rcp_f32_e32 v169, v169\n" \
  "v_rcp_f32_e32 v170, v170\n" \
  "v_rcp_f32_e32 v171, v171\n" \
  "s_setprio 0\n" \
  "v_mul_f32_e32 v156, v104, v156\n" \
  "v_mul_f32_e32 v157, v105, v157\n" \
  "v_mul_f32_e32 v158, v106, v158\n" \
  "v_mul_f32_e32 v159, v107, v159\n" \
  "v_mul_f32_e32 v160, v108, v160\n" \
  "v_mul_f32_e32 v161, v109, v161\n" \
  "v_mul_f32_e32 v162, v110, v162\n" \
  "v_mul_f32_e32 v163, v111, v163\n" \
  "v_mul_f32_e32 v164, v112, v164\n" \
  "v_mul_f32_e32 v165, v113, v165\n" \
  "v_mul_f32_e32 v166, v114, v166\n" \
  "v_mul_f32_e32 v167, v115, v167\n" \
  "v_mul_f32_e32 v168, v116, v168\n" \
  "v_mul_f32_e32 v169, v117, v169\n" \
  "v_mul_f32_e32 v170, v118, v170\n" \
  "v_mul_f32_e32 v171, v119, v171\n" \
  "s_sub_i32 %[rem], %[rem], 1\n" \
  "s_cmp_gt_i32 %[rem], 0\n" \
  "s_cbranch_scc0 END_%=\n" \
  "s_setprio 1\n" \
  "s_waitcnt lgkmcnt(0)\n" \
  "v_mfma_f32_32x32x16_bf16 v[104:119], v[140:143], v[44:47], 0\n" \
  "s_waitcnt vmcnt(5)\n" \
  "v_cvt_pk_bf16_f32 v172, v156, v157\n" \
  "v_cvt_pk_bf16_f32 v173, v158, v159\n" \
  "s_cmp_gt_i32 %[rem], 2\n" \
  "s_cbranch_scc1 X5_%=\n" \
  "s_waitcnt vmcnt(0)\n" \
  "X5_%=:\n" \
  "v_cvt_pk_bf16_f32 v174, v160, v161\n" \
  "v_cvt_pk_bf16_f32 v175, v162, v163\n" \
  "v_cvt_pk_bf16_f32 v176, v164, v165\n" \
  "v_cvt_pk_bf16_f32 v177, v166, v167\n" \
  "v_cvt_pk_bf16_f32 v178, v168, v169\n" \
  "v_cvt_pk_bf16_f32 v179, v170, v171\n" \
  "v_mad_u32_u16 v190, v180, %[c80], v201 op_sel:[1,0,0,0]\n" \
  "v_mfma_f32_32x32x16_bf16 v[104:119], v[144:147], v[52:55], v[104:119]\n" \
  "ds_read_b128 v[140:143], v190\n" \
  "ds_read_b128 v[144:147], v190 offset:32\n" \
  "v_exp_f32_e32 v156, v88\n" \
  "v_exp_f32_e32 v157, v89\n" \
  "v_exp_f32_e32 v158, v90\n" \
  "v_mfma_f32_32x32x16_bf16 v[72:87], v[172:175], v[148:151], v[72:87]\n" \
  "v_mad_u32_u16 v191, v182, %[c80], v202 op_sel:[1,0,0,0]\n" \
  "v_mad_u32_u16 v192, v183, %[c80], v202 op_sel:[1,0,0,0]\n" \
  "ds_read_b64_tr_b16 v[148:149], v191\n" \
  "ds_read_b64_tr_b16 v[150:151], v192\n" \
  "v_exp_f32_e32 v159, v91\n" \
  "v_exp_f32_e32 v160, v92\n" \
  "v_mfma_f32_32x32x16_bf16 v[72:87], v[176:179], v[152:155], v[72:87]\n" \
  "v_mad_u32_u16 v193, v184, %[c80], v202 op_sel:[1,0,0,0]\n" \
  "v_mad_u32_u16 v194, v185, %[c80], v202 op_sel:[1,0,0,0]\n" \
  "ds_read_b64_tr_b16 v[152:153], v193\n" \
  "ds_read_b64_tr_b16 v[154:155], v194\n" \
  "v_exp_f32_e32 v161, v93\n" \
  "v_exp_f32_e32 v162, v94\n" \
  "v_mfma_f32_32x32x16_bf16 v[104:119], v[136:139], v[36:39], v[104:119]\n" \
  "ds_read_b128 v[136:139], v200\n" \
  "ds_read_b128 v[186:189], v207 offset:1024\n" \
  "ds_read_b32 v181, v208 offset:1024\n" \
  "v_exp_f32_e32 v163, v95\n" \
  "v_exp_f32_e32 v164, v96\n" \
  "v_exp_f32_e32 v165, v97\n" \
  "v_exp_f32_e32 v166, v98\n" \
  "v_exp_f32_e32 v167, v99\n" \
  "v_exp_f32_e32 v168, v100\n" \
  "v_exp_f32_e32 v169, v101\n" \
  "v_exp_f32_e32 v170, v102\n" \
  "v_exp_f32_e32 v171, v103\n" \
  "s_setprio 0\n" \
  "v_add_f32_e32 v156, 1.0, v156\n" \
  "v_add_f32_e32 v157, 1.0, v157\n" \
  "v_add_f32_e32 v158, 1.0, v158\n" \
  "v_add_f32_e32 v159, 1.0, v159\n" \
  "v_add_f32_e32 v160, 1.0, v160\n" \
  "v_add_f32_e32 v161, 1.0, v161\n" \
  "v_add_f32_e32 v162, 1.0, v162\n" \
  "v_add_f32_e32 v163, 1.0, v163\n" \
  "v_add_f32_e32 v164, 1.0, v164\n" \
  "v_add_f32_e32 v165, 1.0, v165\n" \
  "v_add_f32_e32 v166, 1.0, v166\n" \
  "v_add_f32_e32 v167, 1.0, v167\n" \
  "v_add_f32_e32 v168, 1.0, v168\n" \
  "v_add_f32_e32 v169, 1.0, v169\n" \
  "v_add_f32_e32 v170, 1.0, v170\n" \
  "v_add_f32_e32 v171, 1.0, v171\n" \
  "s_setprio 1\n" \
  "v_mfma_f32_32x32x16_bf16 v[104:119], v[120:123], v[4:7], v[104:119]\n" \
  "ds_read_b128 v[120:123], v196\n" \
  "v_rcp_f32_e32 v156, v156\n" \
  "v_rcp_f32_e32 v157, v157\n" \
  "v_rcp_f32_e32 v158, v158\n" \
  "v_mfma_f32_32x32x16_bf16 v[104:119], v[124:127], v[12:15], v[104:119]\n" \
  "ds_read_b128 v[124:127], v197\n" \
  "v_rcp_f32_e32 v159, v159\n" \
  "v_rcp_f32_e32 v160, v160\n" \
  "v_rcp_f32_e32 v161, v161\n" \
  "v_mfma_f32_32x32x16_bf16 v[104:119], v[128:131], v[20:23], v[104:119]\n" \
  "ds_read_b128 v[128:131], v198\n" \
  "v_rcp_f32_e32 v162, v162\n" \
  "v_rcp_f32_e32 v163, v163\n" \
  "v_rcp_f32_e32 v164, v164\n" \
  "v_mfma_f32_32x32x16_bf16 v[104:119], v[132:135], v[28:31], v[104:119]\n" \
  "ds_read_b128 v[132:135], v199\n" \
  "v_rcp_f32_e32 v165, v165\n" \
  "v_rcp_f32_e32 v166, v166\n" \
  "v_rcp_f32_e32 v167, v167\n" \
  "v_rcp_f32_e32 v168, v168\n" \
  "v_rcp_f32_e32 v169, v169\n" \
  "v_rcp_f32_e32 v170, v170\n" \
  "v_rcp_f32_e32 v171, v171\n" \
  "s_setprio 0\n" \
  "v_mul_f32_e32 v156, v88, v156\n" \
  "v_mul_f32_e32 v157, v89, v157\n" \
  "v_mul_f32_e32 v158, v90, v158\n" \
  "v_mul_f32_e32 v159, v91, v159\n" \
  "v_mul_f32_e32 v160, v92, v160\n" \
  "v_mul_f32_e32 v161, v93, v161\n" \
  "v_mul_f32_e32 v162, v94, v162\n" \
  "v_mul_f32_e32 v163, v95, v163\n" \
  "v_mul_f32_e32 v164, v96, v164\n" \
  "v_mul_f32_e32 v165, v97, v165\n" \
  "v_mul_f32_e32 v166, v98, v166\n" \
  "v_mul_f32_e32 v167, v99, v167\n" \
  "v_mul_f32_e32 v168, v100, v168\n" \
  "v_mul_f32_e32 v169, v101, v169\n" \
  "v_mul_f32_e32 v170, v102, v170\n" \
  "v_mul_f32_e32 v171, v103, v171\n" \
  "s_setprio 1\n" \
  "s_waitcnt lgkmcnt(0)\n" \
  "v_mfma_f32_32x32x16_bf16 v[88:103], v[140:143], v[40:43], 0\n" \
  "v_cvt_pk_bf16_f32 v172, v156, v157\n" \
  "v_cvt_pk_bf16_f32 v173, v158, v159\n" \
  "v_cvt_pk_bf16_f32 v174, v160, v161\n" \
  "v_cvt_pk_bf16_f32 v175, v162, v163\n" \
  "v_cvt_pk_bf16_f32 v176, v164, v165\n" \
  "v_cvt_pk_bf16_f32 v177, v166, v167\n" \
  "v_cvt_pk_bf16_f32 v178, v168, v169\n" \
  "v_cvt_pk_bf16_f32 v179, v170, v171\n" \
  "v_mfma_f32_32x32x16_bf16 v[88:103], v[144:147], v[48:51], v[88:103]\n" \
  "ds_read_b32 v180, v205 offset:768\n" \
  "ds_read_b128 v[182:185], v206 offset:512\n" \
  "v_exp_f32_e32 v156, v104\n" \
  "v_exp_f32_e32 v157, v105\n" \
  "v_exp_f32_e32 v158, v106\n" \
  "v_mfma_f32_32x32x16_bf16 v[56:71], v[172:175], v[148:151], v[56:71]\n" \
  "v_exp_f32_e32 v159, v107\n" \
  "v_exp_f32_e32 v160, v108\n" \
  "v_exp_f32_e32 v161, v109\n" \
  "v_mfma_f32_32x32x16_bf16 v[56:71], v[176:179], v[152:155], v[56:71]\n" \
  "v_exp_f32_e32 v162, v110\n" \
  "v_exp_f32_e32 v163, v111\n" \
  "v_exp_f32_e32 v164, v112\n" \
  "v_mfma_f32_32x32x16_bf16 v[88:103], v[136:139], v[32:35], v[88:103]\n" \
  "s_cmp_gt_i32 %[rem], 3\n" \
  "s_cbranch_scc0 X6_%=\n" \
  "s_add_u32 m0, %[stg], 4096\n" \
  "v_add_u32 v190, v181, v209\n" \
  "buffer_load_dwordx4 v190, %[re], 0 offen lds\n" \
  "s_add_u32 m0, %[stg], 0\n" \
  "v_mad_u32_u16 v191, v186, %[c128], v203\n" \
  "buffer_load_dwordx4 v191, %[rx], 0 offen lds\n" \
  "s_add_u32 m0, %[stg], 1024\n" \
  "v_mad_u32_u16 v192, v187, %[c128], v204\n" \
  "buffer_load_dwordx4 v192, %[rx], 0 offen lds\n" \
  "s_add_u32 m0, %[stg], 2048\n" \
  "v_mad_u32_u16 v193, v188, %[c128], v203\n" \
  "buffer_load_dwordx4 v193, %[rx], 0 offen lds\n" \
  "s_add_u32 m0, %[stg], 3072\n" \
  "v_mad_u32_u16 v194, v189, %[c128], v204\n" \
  "buffer_load_dwordx4 v194, %[rx], 0 offen lds\n" \
  "X6_%=:\n" \
  "v_exp_f32_e32 v165, v113\n" \
  "v_exp_f32_e32 v166, v114\n" \
  "v_exp_f32_e32 v167, v115\n" \
  "v_exp_f32_e32 v168, v116\n" \
  "v_exp_f32_e32 v169, v117\n" \
  "v_exp_f32_e32 v170, v118\n" \
  "v_exp_f32_e32 v171, v119\n" \
  "s_setprio 0\n" \
  "v_add_f32_e32 v156, 1.0, v156\n" \
  "v_add_f32_e32 v157, 1.0, v157\n" \
  "v_add_f32_e32 v158, 1.0, v158\n" \
  "v_add_f32_e32 v159, 1.0, v159\n" \
  "v_add_f32_e32 v160, 1.0, v160\n" \
  "v_add_f32_e32 v161, 1.0, v161\n" \
  "v_add_f32_e32 v162, 1.0, v162\n" \
  "v_add_f32_e32 v163, 1.0, v163\n" \
  "v_add_f32_e32 v164, 1.0, v164\n" \
  "v_add_f32_e32 v165, 1.0, v165\n" \
  "v_add_f32_e32 v166, 1.0, v166\n" \
  "v_add_f32_e32 v167, 1.0, v167\n" \
  "v_add_f32_e32 v168, 1.0, v168\n" \
  "v_add_f32_e32 v169, 1.0, v169\n" \
  "v_add_f32_e32 v170, 1.0, v170\n" \
  "v_add_f32_e32 v171, 1.0, v171\n" \
  "s_setprio 1\n" \
  "v_mfma_f32_32x32x16_bf16 v[88:103], v[120:123], v[0:3], v[88:103]\n" \
  "v_rcp_f32_e32 v156, v156\n" \
  "v_rcp_f32_e32 v157, v157\n" \
  "v_rcp_f32_e32 v158, v158\n" \
  "v_mfma_f32_32x32x16_bf16 v[88:103], v[124:127], v[8:11], v[88:103]\n" \
  "v_rcp_f32_e32 v159, v159\n" \
  "v_rcp_f32_e32 v160, v160\n" \
  "v_rcp_f32_e32 v161, v161\n" \
  "v_mfma_f32_32x32x16_bf16 v[88:103], v[128:131], v[16:19], v[88:103]\n" \
  "v_rcp_f32_e32 v162, v162\n" \
  "v_rcp_f32_e32 v163, v163\n" \
  "v_rcp_f32_e32 v164, v164\n" \
  "v_mfma_f32_32x32x16_bf16 v[88:103], v[132:135], v[24:27], v[88:103]\n" \
  "v_rcp_f32_e32 v165, v165\n" \
  "v_rcp_f32_e32 v166, v166\n" \
  "v_rcp_f32_e32 v167, v167\n" \
  "v_rcp_f32_e32 v168, v168\n" \
  "v_rcp_f32_e32 v169, v169\n" \
  "v_rcp_f32_e32 v170, v170\n" \
  "v_rcp_f32_e32 v171, v171\n" \
  "s_setprio 0\n" \
  "v_mul_f32_e32 v156, v104, v156\n" \
  "v_mul_f32_e32 v157, v105, v157\n" \
  "v_mul_f32_e32 v158, v106, v158\n" \
  "v_mul_f32_e32 v159, v107, v159\n" \
  "v_mul_f32_e32 v160, v108, v160\n" \
  "v_mul_f32_e32 v161, v109, v161\n" \
  "v_mul_f32_e32 v162, v110, v162\n" \
  "v_mul_f32_e32 v163, v111, v163\n" \
  "v_mul_f32_e32 v164, v112, v164\n" \
  "v_mul_f32_e32 v165, v113, v165\n" \
  "v_mul_f32_e32 v166, v114, v166\n" \
  "v_mul_f32_e32 v167, v115, v167\n" \
  "v_mul_f32_e32 v168, v116, v168\n" \
  "v_mul_f32_e32 v169, v117, v169\n" \
  "v_mul_f32_e32 v170, v118, v170\n" \
  "v_mul_f32_e32 v171, v119, v171\n" \
  "v_add_u32_e32 v205, 0x200, v205\n" \
  "v_add_u32_e32 v206, 0x200, v206\n" \
  "v_add_u32_e32 v207, 0x200, v207\n" \
  "v_add_u32_e32 v208, 0x200, v208\n" \
  "s_sub_i32 %[rem], %[rem], 1\n" \
  "s_cmp_gt_i32 %[rem], 0\n" \
  "s_cbranch_scc1 LOOP_%=\n" \
  "END_%=:\n" \
  "v_cvt_pk_bf16_f32 v172, v156, v157\n" \
  "v_cvt_pk_bf16_f32 v173, v158, v159\n" \
  "v_cvt_pk_bf16_f32 v174, v160, v161\n" \
  "v_cvt_pk_bf16_f32 v175, v162, v163\n" \
  "v_cvt_pk_bf16_f32 v176, v164, v165\n" \
  "v_cvt_pk_bf16_f32 v177, v166, v167\n" \
  "v_cvt_pk_bf16_f32 v178, v168, v169\n" \
  "v_cvt_pk_bf16_f32 v179, v170, v171\n" \
  "s_nop 1\n" \
  "v_mfma_f32_32x32x16_bf16 v[72:87], v[172:175], v[148:151], v[72:87]\n" \
  "v_mfma_f32_32x32x16_bf16 v[72:87], v[176:179], v[152:155], v[72:87]\n" \
  "s_waitcnt lgkmcnt(0)\n" \
  "s_nop 15\n"
#define XLOOP_CLOB "v88", "v89", "v90", "v91", "v92", "v93", "v94", "v95", "v96", "v97", "v98", "v99", "v100", "v101", "v102", "v103", "v104", "v105", "v106", "v107", "v108", "v109", "v110", "v111", "v112", "v113", "v114", "v115", "v116", "v117", "v118", "v119", "v120", "v121", "v122", "v123", "v124", "v125", "v126", "v127", "v128", "v129", "v130", "v131", "v132", "v133", "v134", "v135", "v136", "v137", "v138", "v139", "v140", "v141", "v142", "v143", "v144", "v145", "v146", "v147", "v148", "v149", "v150", "v151", "v152", "v153", "v154", "v155", "v156", "v157", "v158", "v159", "v160", "v161", "v162", "v163", "v164", "v165", "v166", "v167", "v168", "v169", "v170", "v171", "v172", "v173", "v174", "v175", "v176", "v177", "v178", "v179", "v180", "v181", "v182", "v183", "v184", "v185", "v186", "v187", "v188", "v189", "v190", "v191", "v192", "v193", "v194", "v195"

constexpr int CAPG = 1024;
constexpr int K2_ENT = 0;
constexpr int K2_EAD = K2_ENT + 4 * CAPG * 4;
constexpr int K2_XOWN = K2_EAD + 4 * CAPG * 4;
constexpr int K2_W = K2_XOWN + 16384;
constexpr int K2_LUT = K2_W + 18432;
constexpr int K2_CNT = K2_LUT + 2688;
constexpr int K2_ST = K2_CNT + 128;
constexpr int STW = 2 * 5120;
constexpr int K2_LDS = K2_ST + 8 * STW;
static_assert(OFFROW * 2 == 2176 && K2_LDS <= 163840 && K2_EAD - K2_ENT == 16384 && K2_ST - K2_LUT == 176 * 16, "lds");

DI bf16x8 asb(u32x4 v) { return __builtin_bit_cast(bf16x8, v); }
DI u32x4 pack8(const f32x16& x, int s, float sc) {
    u32x4 o;
#pragma unroll
    for (int j = 0; j < 4; j++) o[j] = pk2(x[8 * s + 2 * j] * sc, x[8 * s + 2 * j + 1] * sc);
    return o;
}
typedef __attribute__((ext_vector_type(16))) unsigned u32x16;
typedef __attribute__((ext_vector_type(8))) unsigned u32x8;
DI u32x8 cat2(u32x4 a, u32x4 b) { return __builtin_shufflevector(a, b, 0, 1, 2, 3, 4, 5, 6, 7); }
DI u32x16 cat4(u32x4 a, u32x4 b, u32x4 c, u32x4 d) { return __builtin_shufflevector(cat2(a, b), cat2(c, d), 0, 1, 2, 3, 4, 5, 6, 7, 8, 9, 10, 11, 12, 13, 14, 15); }

DI unsigned opq(unsigned v) { asm volatile("" : "+v"(v)); return v; }

__global__ __launch_bounds__(512) void k2(const char* __restrict__ x, char* __restrict__ ws, char* __restrict__ out) {
    __shared__ __attribute__((aligned(16))) char lds[K2_LDS];
    STAMP(1, 0);
    const unsigned L = ldsaddr(lds);
    const unsigned tid = threadIdx.x;
    const int wv = __builtin_amdgcn_readfirstlane(tid >> 6);
    const int b = (blockIdx.x & 7) * 32 + (blockIdx.x >> 3);
    unsigned pos[2], rem[2];
    u32x2 t0, t1;
    asm volatile("buffer_load_dwordx2 %0, %2, %3, 0 offen\n\tbuffer_load_dwordx2 %1, %2, %3, 0 offen offset:2176"
                 : "=&v"(t0), "=&v"(t1) : "v"((tid >> 2) * (OFFROW * 4) + b * 8 + (tid & 2) * 2), "s"(mkrsrc(ws + WS_OFF, NCH * OFFROW * 2)) : "memory");
    if (tid < 176) ldsw<u32x4>(L + K2_LUT + tid * 16, u32x4{0, 0, 0, 0});
    lds_barrier();
    if (tid < 32) ldsw<unsigned short>(L + K2_LUT + tid * 82, 0x3F80);
    const int grp = (wv + (wv >> 2)) & 3, half = wv >> 2;
    const int node0 = gstart(b * 4 + grp), nnode = gstart(b * 4 + grp + 1) - node0;
    const u32x4 rx = mkrsrc(x, NN * 128);
    const u32x4 rw = mkrsrc(ws + WS_W, 34 * 1024);
    const u32x4 re = mkrsrc(ws + WS_E, NCH * CH * 32);
    const u32x4 ri = mkrsrc(ws + WS_ID, NCH * CH * 4);
    const unsigned stg = L + K2_ST + wv * STW;

    u32x16 w1a, w1b, qf;
    u32x8 w3;
    f32x16 ag0, ag1;
    bool first = true, pass0 = true;
    while (true) {
        bool more;
        do {
            if (first) {
                asm volatile("s_waitcnt vmcnt(0)" : "+v"(t0), "+v"(t1)::"memory");
                const unsigned o0 = __builtin_amdgcn_alignbit(t0[1], t0[0], tid * 16), o1 = __builtin_amdgcn_alignbit(t1[1], t1[0], tid * 16);
                const unsigned cb = (tid >> 2) * (2 * CH);
                pos[0] = cb + (o0 & 0xffff); rem[0] = (o0 >> 16) - (o0 & 0xffff);
                pos[1] = cb + CH + (o1 & 0xffff); rem[1] = (o1 >> 16) - (o1 & 0xffff);
                STAMP(1, 1);
            }
            u32x4 a[2][3];
            unsigned req[2];
#pragma unroll
            for (int k = 0; k < 2; k++) req[k] = min(rem[k], 12u);
            asm volatile(
                "buffer_load_dwordx4 %0, %6, %12, 0 offen\n\tbuffer_load_dwordx4 %1, %7, %12, 0 offen\n\t"
                "buffer_load_dwordx4 %2, %8, %12, 0 offen offset:16\n\tbuffer_load_dwordx4 %3, %9, %12, 0 offen offset:16\n\t"
                "buffer_load_dwordx4 %4, %10, %12, 0 offen offset:32\n\tbuffer_load_dwordx4 %5, %11, %12, 0 offen offset:32"
                : "=&v"(a[0][0]), "=&v"(a[1][0]), "=&v"(a[0][1]), "=&v"(a[1][1]), "=&v"(a[0][2]), "=&v"(a[1][2])
                : "v"(req[0] > 0 ? pos[0] * 4 : 0x80000000u), "v"(req[1] > 0 ? pos[1] * 4 : 0x80000000u),
                  "v"(req[0] > 4 ? pos[0] * 4 : 0x80000000u), "v"(req[1] > 4 ? pos[1] * 4 : 0x80000000u),
                  "v"(req[0] > 8 ? pos[0] * 4 : 0x80000000u), "v"(req[1] > 8 ? pos[1] * 4 : 0x80000000u), "s"(ri)
                : "memory");
            const unsigned lane = opq(tid) & 63, part = lane & 3;
            if (first) {
                const unsigned xc0 = 16 * ((lane & 7) ^ (lane >> 4));
#pragma unroll
                for (int i = 0; i < 2; i++) {
                    const int pc = half * 2 + i, row = 8 * pc + (lane >> 3);
                    dma16(rx, L + K2_XOWN + grp * 4096 + pc * 1024, row < nnode ? (node0 + row) * 128 + (xc0 ^ (64 * i)) : 0x80000000u);
                }
                dma16(rw, L + K2_W + wv * 1024, wv * 1024 + lane * 16);
                dma16(rw, L + K2_W + wv * 1024 + 8192, wv * 1024 + 8192 + lane * 16);
                if (wv < 2) dma16(rw, L + K2_W + wv * 1024 + 16384, wv * 1024 + 16384 + lane * 16);
            }
            const unsigned rq = req[0] + req[1];
            unsigned inc = rq;
            inc += dpp0<0x114>(inc); inc += dpp0<0x118>(inc);
            unsigned rt = rq;
            rt += dpp0<0x128>(rt); rt += dpp0<0x124>(rt);
            const auto p16 = __builtin_amdgcn_permlane16_swap(rt, rt, false, false);
            const unsigned s2 = p16[0] + p16[1];
            const auto p32 = __builtin_amdgcn_permlane32_swap(s2, s2, false, false);
            inc += ((lane & 16) ? p16[0] : 0u) + ((lane & 32) ? p32[0] : 0u);
            unsigned old = 0;
            if (lane < 4) old = __hip_atomic_fetch_add((LDS unsigned*)(size_t)(L + K2_CNT + lane * 4), p32[0] + p32[1], __ATOMIC_RELAXED, __HIP_MEMORY_SCOPE_WORKGROUP);
            const unsigned o0 = __builtin_amdgcn_readlane(old, 0), o1 = __builtin_amdgcn_readlane(old, 1), o2 = __builtin_amdgcn_readlane(old, 2), o3 = __builtin_amdgcn_readlane(old, 3);
            unsigned sl = inc - rq + ((part & 2) ? ((part & 1) ? o3 : o2) : ((part & 1) ? o1 : o0));
            if (!first) WAITVM(0);
            asm volatile("s_waitcnt vmcnt(4)" : "+v"(a[0][0]), "+v"(a[1][0]), "+v"(a[0][1]), "+v"(a[1][1]), "+v"(a[0][2]), "+v"(a[1][2])::"memory");
            STAMP(1, 5);
            bool full = false;
#pragma unroll
            for (int k = 0; k < 2; k++) {
                const unsigned gr = min(req[k], (unsigned)CAPG - min(sl, (unsigned)CAPG));
                const unsigned ad = L + K2_ENT + (part * CAPG + sl) * 4;
                unsigned ea = pos[k] * 32;
                asm volatile(
#define STEP(j, R) "v_cmpx_lt_u32 vcc, " #j ", %1\n\tds_write_b32 %2, " R " offset:4*" #j "\n\tds_write_b32 %2, %0 offset:16384+4*" #j "\n\tv_add_u32 %0, 32, %0\n\t"
                    STEP(0, "%3") STEP(1, "%4") STEP(2, "%5") STEP(3, "%6") STEP(4, "%7") STEP(5, "%8") STEP(6, "%9")
                    "s_cbranch_execz 1f\n\t" STEP(7, "%10") STEP(8, "%11") "s_cbranch_execz 1f\n\t" STEP(9, "%12") STEP(10, "%13") STEP(11, "%14")
                    "1:\n\ts_mov_b64 exec, -1"
                    : "+v"(ea)
                    : "v"(gr), "v"(ad), "v"(a[k][0][0]), "v"(a[k][0][1]), "v"(a[k][0][2]), "v"(a[k][0][3]), "v"(a[k][1][0]), "v"(a[k][1][1]),
                      "v"(a[k][1][2]), "v"(a[k][1][3]), "v"(a[k][2][0]), "v"(a[k][2][1]), "v"(a[k][2][2]), "v"(a[k][2][3])
                    : "memory", "vcc");
                full |= gr < req[k];
                sl += req[k]; rem[k] -= gr; pos[k] += gr;
            }
            more = __any((rem[0] | rem[1]) != 0 && !full);
            first = false;
        } while (more);
        if ((rem[0] | rem[1]) != 0) ldsw<unsigned>(L + K2_CNT + 16, 1u);
        lds_barrier();
        STAMP(1, 2);
        const unsigned lane = opq(tid) & 63, r = lane & 31, h = lane >> 5;
        const unsigned gcnt = min((unsigned)__builtin_amdgcn_readfirstlane(ldsr<unsigned>(L + K2_CNT + grp * 4)), (unsigned)CAPG);
        const unsigned again = __builtin_amdgcn_readfirstlane(ldsr<unsigned>(L + K2_CNT + 16));
        const int ntile = (gcnt + 31) >> 5;
        int nt = (ntile - half + 1) >> 1;
        const unsigned lb = L + K2_ENT + grp * CAPG * 4;
        if (gcnt + r < (unsigned)ntile * 32) ldsw<unsigned>(lb + (gcnt + r) * 4 + h * (K2_EAD - K2_ENT), h ? 0x7fffffe0u : 0x0020ffffu);
        unsigned pe4 = lb + half * 128 + (lane >> 3) * 16, pea = lb + (K2_EAD - K2_ENT) + half * 128 + (lane >> 1) * 4;
        const unsigned low = (lane & 1) * 16;
        const unsigned xc0 = 16 * ((lane & 7) ^ (lane >> 4)), xc1 = xc0 ^ 64;
        {
            const u32x4 ea4 = ldsr<u32x4>(pe4), eb4 = ldsr<u32x4>(pe4 + 256);
            const unsigned eaa = ldsr<unsigned>(pea) + low, eab = ldsr<unsigned>(pea + 256) + low;
            if (nt > 0) {
                dma16(re, stg + 4096, eaa);
                dma16(rx, stg, mad16lo(ea4[0], 128, xc0)); dma16(rx, stg + 1024, mad16lo(ea4[1], 128, xc1));
                dma16(rx, stg + 2048, mad16lo(ea4[2], 128, xc0)); dma16(rx, stg + 3072, mad16lo(ea4[3], 128, xc1));
            }
            if (nt > 1) {
                dma16(re, stg + 5120 + 4096, eab);
                dma16(rx, stg + 5120, mad16lo(eb4[0], 128, xc0)); dma16(rx, stg + 5120 + 1024, mad16lo(eb4[1], 128, xc1));
                dma16(rx, stg + 5120 + 2048, mad16lo(eb4[2], 128, xc0)); dma16(rx, stg + 5120 + 3072, mad16lo(eb4[3], 128, xc1));
            }
        }
        if (pass0) {
            WAITVM(10);
            if (nt < 2) WAITVM(0);
            lds_barrier();
            STAMP(1, 6);
            const unsigned wb = L + K2_W + lane * 16;
            w1a = cat4(ldsr<u32x4>(wb), ldsr<u32x4>(wb + 1024), ldsr<u32x4>(wb + 2048), ldsr<u32x4>(wb + 3072));
            w1b = cat4(ldsr<u32x4>(wb + 4096), ldsr<u32x4>(wb + 5120), ldsr<u32x4>(wb + 6144), ldsr<u32x4>(wb + 7168));
            w3 = cat2(ldsr<u32x4>(wb + 8192), ldsr<u32x4>(wb + 9216));
            const unsigned nd = (r & 19) | ((r & 4) << 1) | ((r & 8) >> 1);
            f32x16 q0 = {0}, q1 = {0};
#pragma unroll
            for (int s = 0; s < 4; s++) {
                const bf16x8 a = asb(ldsr<u32x4>(L + K2_XOWN + grp * 4096 + nd * 128 + 16 * ((2 * s + h) ^ ((nd >> 1) & 7))));
                q0 = MFMA(a, asb(ldsr<u32x4>(wb + (10 + s * 2) * 1024)), q0);
                q1 = MFMA(a, asb(ldsr<u32x4>(wb + (11 + s * 2) * 1024)), q1);
            }
            lds_barrier();
#pragma unroll
            for (int i = 0; i < 2; i++) dma16(rw, L + K2_W + (wv * 2 + i) * 1024, (18 + wv * 2 + i) * 1024 + lane * 16);
            const float sc = -1.4426950408889634f;
            qf = cat4(pack8(q0, 0, sc), pack8(q1, 0, sc), pack8(q0, 1, sc), pack8(q1, 1, sc));
#pragma unroll
            for (int i = 0; i < 16; i++) { ag0[i] = 0.f; ag1[i] = 0.f; }
            pass0 = false;
            STAMP(1, 7);
        }
        if (nt > 0) {
            const unsigned xrd = r * 128 + 16 * (h ^ ((r >> 1) & 7));
            const unsigned jr = 4 * (r & 7) + (r >> 3);
            const u32x4 xr4 = {stg + xrd, stg + (xrd ^ 32), stg + (xrd ^ 64), stg + (xrd ^ 96)};
            unsigned pj = lb + half * 128 + jr * 4, pv = lb + half * 128 + 16 * (4 * h + ((lane >> 2) & 3));
            asm volatile(XLOOP_ASM
                         : "+{v[56:71]}"(ag0), "+{v[72:87]}"(ag1), "+{v205}"(pj), "+{v206}"(pv), "+{v207}"(pe4), "+{v208}"(pea), [rem] "+s"(nt)
                         : "{v[0:15]}"(w1a), "{v[16:31]}"(w1b), "{v[32:39]}"(w3), "{v[40:55]}"(qf), "{v[196:199]}"(xr4),
                           "{v200}"(stg + 4096 + jr * 32 + 16 * h), "{v201}"(L + K2_LUT + 16 * h),
                           "{v202}"(L + K2_LUT + ((lane >> 4) & 1) * 32 + (lane & 3) * 8), "{v203}"(xc0), "{v204}"(xc1), "{v209}"(low),
                           [rx] "s"(rx), [re] "s"(re), [stg] "s"(stg), [c80] "s"(80), [c128] "s"(128)
                         : "memory", "scc", XLOOP_CLOB);
        }
        WAITVM(0);
        if (!again) break;
        lds_barrier();
        if (tid < 8) ldsw<unsigned>(L + K2_CNT + tid * 4, 0u);
        lds_barrier();
    }
    STAMP(1, 3);
    {
        const unsigned lane = opq(tid) & 63, r = lane & 31, h = lane >> 5;
        const unsigned xb = stg + lane * 16;
#pragma unroll
        for (int i = 0; i < 4; i++) {
            ldsw<f32x4>(xb + i * 1024, f32x4{ag0[4 * i], ag0[4 * i + 1], ag0[4 * i + 2], ag0[4 * i + 3]});
            ldsw<f32x4>(xb + 4096 + i * 1024, f32x4{ag1[4 * i], ag1[4 * i + 1], ag1[4 * i + 2], ag1[4 * i + 3]});
        }
        lds_barrier();
        const int pw = half ? grp : 4 + ((grp + 3) & 3);
        const unsigned pb = L + K2_ST + pw * STW + lane * 16;
#pragma unroll
        for (int i = 0; i < 4; i++) {
            const f32x4 a = ldsr<f32x4>(pb + i * 1024), c = ldsr<f32x4>(pb + 4096 + i * 1024);
#pragma unroll
            for (int j = 0; j < 4; j++) { ag0[4 * i + j] += a[j]; ag1[4 * i + j] += c[j]; }
        }
        f32x16 y = {0};
        const unsigned wb = L + K2_W + half * 8192 + lane * 16;
        const unsigned xrd = r * 128 + 16 * (h ^ ((r >> 1) & 7));
#pragma unroll
        for (int s = 0; s < 4; s++) y = MFMA(asb(ldsr<u32x4>(wb + s * 1024)), asb(ldsr<u32x4>(L + K2_XOWN + grp * 4096 + (xrd ^ (32 * s)))), y);
        const float nl2 = -0.6931471805599453f;
        y = MFMA(asb(ldsr<u32x4>(wb + 4096)), asb(pack8(ag0, 0, nl2)), y);
        y = MFMA(asb(ldsr<u32x4>(wb + 5120)), asb(pack8(ag0, 1, nl2)), y);
        y = MFMA(asb(ldsr<u32x4>(wb + 6144)), asb(pack8(ag1, 0, nl2)), y);
        y = MFMA(asb(ldsr<u32x4>(wb + 7168)), asb(pack8(ag1, 1, nl2)), y);
        if (r < (unsigned)nnode) {
            char* op = out + (size_t)(node0 + r) * 128 + half * 64 + h * 8;
#pragma unroll
            for (int i = 0; i < 4; i++) {
                float v[4];
#pragma unroll
                for (int j = 0; j < 4; j++) {
                    const float a = y[4 * i + j];
                    v[j] = a * __builtin_amdgcn_rcpf(1.0f + __builtin_amdgcn_exp2f(a * -1.4426950408889634f));
                }
                *(u32x2*)(op + i * 16) = u32x2{pk2(v[0], v[1]), pk2(v[2], v[3])};
            }
        }
    }
    STAMP(1, 4);
}

extern "C" void kernel_launch(void* const* d_in, const int* in_sizes, int n_in, void* d_out, int out_size, void* d_ws, size_t ws_size,
                              hipStream_t stream) {
    k1<<<NCH, 1024, 0, stream>>>((const int*)d_in[2], (const int*)d_in[3], (const char*)d_in[1], (const unsigned short*)d_in[4],
                                 (const unsigned short*)d_in[5], (char*)d_ws);
    k2<<<256, 512, 0, stream>>>((const char*)d_in[0], (char*)d_ws, (char*)d_out);
}
```

```cpp
#define A 1
#include <hip/hip_runtime.h>
#include <hip/hip_bf16.h>
#include <stdint.h>
#include <type_traits>

typedef __attribute__((ext_vector_type(16))) unsigned u32x16;
typedef __attribute__((ext_vector_type(8))) unsigned u32x8;
typedef __attribute__((ext_vector_type(4))) unsigned u32x4;
typedef __attribute__((ext_vector_type(2))) unsigned u32x2;
typedef __attribute__((ext_vector_type(8))) short bf16x8;
typedef __attribute__((ext_vector_type(16))) float f32x16;
typedef __attribute__((ext_vector_type(4))) float f32x4;
typedef __attribute__((ext_vector_type(2))) float f32x2;
typedef __attribute__((ext_vector_type(2))) __bf16 bf16x2;
#define LDS __attribute__((address_space(3)))
#define GL __attribute__((address_space(1)))
#define DI __device__ __forceinline__
#define MFMA(a, b, c) __builtin_amdgcn_mfma_f32_32x32x16_bf16((a), (b), (c), 0, 0, 0)

constexpr int NN = 25000, EE = 800000;
constexpr int CH = 3125;
constexpr int NCH = 256;
constexpr int IDS = 3168;
#define SPLIT 12500
constexpr int XB = 16384 - SPLIT;
#define PA 3
constexpr int NP = 448 * PA;
constexpr int NR = CH - NP;
#define TLBS 20
#define ENTS
#define CLS 3
#define CW 256
#define FST 256
constexpr int CROW = 4352;
constexpr int LUTS = 144;
constexpr size_t WS_EPOCH = 0;
constexpr size_t WS_FLAG = 4096;
constexpr size_t WS_CELL = WS_FLAG + 256 * FST;
constexpr size_t WS_ID = WS_CELL + (size_t)NCH * CROW;
constexpr size_t WS_P = WS_ID + (size_t)NCH * IDS * 4;
constexpr size_t WS_E = WS_P + (size_t)NN * 128;
constexpr size_t WS_TS = WS_E + (size_t)EE * 32;
static_assert(WS_P % 128 == 0 && WS_E % 128 == 0 && WS_P >= (XB + 24) * 128 && NP + 3 <= 2048 && NR <= 2048, "ws");

DI u32x4 mkrsrc(const void* p, unsigned bytes) {
    uint64_t a = (uint64_t)p;
    u32x4 r;
    r[0] = __builtin_amdgcn_readfirstlane((unsigned)a);
    r[1] = __builtin_amdgcn_readfirstlane((unsigned)(a >> 32) & 0xffffu);
    r[2] = __builtin_amdgcn_readfirstlane(bytes);
    r[3] = 0x00020000u;
    return r;
}
DI void dma16(u32x4 rsrc, unsigned lds, unsigned voff) {
    asm volatile("s_mov_b32 m0, %0\n\ts_nop 0\n\tbuffer_load_dwordx4 %1, %2, 0 offen lds" ::"s"(lds), "v"(voff), "s"(rsrc) : "memory");
}
DI void dma16s(u32x4 rsrc, unsigned lds, unsigned voff) {
    asm volatile("s_mov_b32 m0, %0\n\ts_nop 0\n\tbuffer_load_dwordx4 %1, %2, 0 offen sc1 lds" ::"s"(lds), "v"(voff), "s"(rsrc) : "memory");
}
DI void lds_barrier() { asm volatile("s_waitcnt lgkmcnt(0)\n\ts_barrier" ::: "memory"); }
#define WAITVM(n) asm volatile("s_waitcnt vmcnt(" #n ")" ::: "memory")
DI unsigned ldsaddr(const void* p) { return (unsigned)(size_t)(LDS const char*)p; }
template <class T> DI T ldsr(unsigned a) { return *(LDS const T*)(size_t)a; }
template <class T> DI void ldsw(unsigned a, T v) { *(LDS T*)(size_t)a = v; }
DI float bf2f(unsigned short v) { return __uint_as_float((unsigned)v << 16); }
DI unsigned pk2(float a, float b) { f32x2 v = {a, b}; return __builtin_bit_cast(unsigned, __builtin_convertvector(v, bf16x2)); }
DI unsigned node_of(unsigned u) { const unsigned d = (u * 25000u + 30000u) >> 15; return (d * 85899u) >> 16 == u && d < 25000u ? d : 0x800000u; }
DI unsigned opq(unsigned v) { asm volatile("" : "+v"(v)); return v; }
template <int CTRL> DI unsigned dpp0(unsigned v) { return __builtin_amdgcn_update_dpp(0u, v, CTRL, 0xf, 0xf, true); }
template <int CTRL, int RM> DI unsigned dppm(unsigned v) { return __builtin_amdgcn_update_dpp(0u, v, CTRL, RM, 0xf, false); }
DI unsigned wave_incl_scan(unsigned v) {
    v += dpp0<0x111>(v); v += dpp0<0x112>(v); v += dpp0<0x114>(v); v += dpp0<0x118>(v);
    v += dppm<0x142, 0xa>(v); v += dppm<0x143, 0xc>(v);
    return v;
}

#define STAMP(k, i)
#define L5(i)
#define LS(i)
#define QLOOP_ASM \
  "ds_read_b32 v160, v74\n" \
  "ds_read_b64 v[172:173], v76 offset:64\n" \
  "ds_read_b32 v166, v77 offset:128\n" \
  "v_mov_b32_e32 v48, 0\n" \
  "v_mov_b32_e32 v49, 0\n" \
  "v_mov_b32_e32 v50, 0\n" \
  "v_mov_b32_e32 v51, 0\n" \
  "v_mov_b32_e32 v52, 0\n" \
  "v_mov_b32_e32 v53, 0\n" \
  "v_mov_b32_e32 v54, 0\n" \
  "v_mov_b32_e32 v55, 0\n" \
  "v_mov_b32_e32 v56, 0\n" \
  "v_mov_b32_e32 v57, 0\n" \
  "v_mov_b32_e32 v58, 0\n" \
  "v_mov_b32_e32 v59, 0\n" \
  "v_mov_b32_e32 v60, 0\n" \
  "v_mov_b32_e32 v61, 0\n" \
  "v_mov_b32_e32 v62, 0\n" \
  "v_mov_b32_e32 v63, 0\n" \
  "s_waitcnt lgkmcnt(0)\n" \
  "v_and_b32_e32 v175, 15, v160\n" \
  "v_mad_u32_u24 v175, v175, %[luts], v71\n" \
  "v_mad_u32_u16 v180, v160, 4, v65 op_sel:[1,0,0,0]\n" \
  "v_max_u32_e32 v180, v180, v70\n" \
  "v_and_or_b32 v174, v166, %[m32], v73\n" \
  "v_mad_u32_u16 v176, v172, %[c128], v182\n" \
  "v_mad_u32_u16 v177, v172, %[c128], v183 op_sel:[1,0,0,0]\n" \
  "v_mad_u32_u16 v178, v173, %[c128], v184\n" \
  "v_mad_u32_u16 v179, v173, %[c128], v185 op_sel:[1,0,0,0]\n" \
  "ds_read_b128 v[132:135], v175\n" \
  "ds_read_b32 v160, v74 offset:128\n" \
  "v_mov_b32_e32 v156, 0\n" \
  "v_mov_b32_e32 v162, 1.0\n" \
  "v_mov_b32_e32 v136, 0\n" \
  "v_mov_b32_e32 v186, 0\n" \
  "v_mov_b32_e32 v157, 0\n" \
  "v_mov_b32_e32 v163, 1.0\n" \
  "v_mov_b32_e32 v137, 0\n" \
  "v_mov_b32_e32 v187, 0\n" \
  "v_mov_b32_e32 v158, 0\n" \
  "v_mov_b32_e32 v164, 1.0\n" \
  "v_mov_b32_e32 v138, 0\n" \
  "v_mov_b32_e32 v188, 0\n" \
  "v_mov_b32_e32 v159, 0\n" \
  "v_mov_b32_e32 v165, 1.0\n" \
  "v_mov_b32_e32 v139, 0\n" \
  "v_mov_b32_e32 v189, 0\n" \
  "s_add_u32 m0, %[stg], 4096\n" \
  "s_waitcnt vmcnt(0)\n" \
  "ds_read_b128 v[128:131], v180\n" \
  "ds_read_b128 v[112:115], v66\n" \
  "ds_read_b128 v[116:119], v67\n" \
  "ds_read_b128 v[120:123], v68\n" \
  "ds_read_b128 v[124:127], v69\n" \
  "s_waitcnt lgkmcnt(0)\n" \
  "buffer_load_dwordx4 v174, %[re], %[np32] offen lds\n" \
  "s_mov_b32 m0, %[stg]\n" \
  "v_mfma_f32_32x32x16_bf16 v[80:95], v[132:135], v[40:43], 0\n" \
  "buffer_load_dwordx4 v176, %[rx], 0 offen sc1 lds\n" \
  "v_mfma_f32_32x32x16_bf16 v[80:95], v[128:131], v[32:35], v[80:95]\n" \
  "buffer_load_dwordx4 v177, %[rx], 0 offen offset:1024 sc1 lds\n" \
  "v_mfma_f32_32x32x16_bf16 v[80:95], v[112:115], v[24:27], v[80:95]\n" \
  "buffer_load_dwordx4 v178, %[rx], 0 offen offset:2048 sc1 lds\n" \
  "v_mfma_f32_32x32x16_bf16 v[80:95], v[120:123], v[28:31], v[80:95]\n" \
  "buffer_load_dwordx4 v179, %[rx], 0 offen offset:3072 sc1 lds\n" \
  "s_sub_u32 %[rem], %[rem], 1\n" \
  "LOOP_%=:\n" \
  "s_setprio 1\n" \
  "v_mfma_f32_32x32x16_bf16 v[96:111], v[132:135], v[44:47], 0\n" \
  "ds_read_b64 v[172:173], v76 offset:128\n" \
  "v_and_b32_e32 v175, 15, v160\n" \
  "v_mad_u32_u24 v175, v175, %[luts], v71\n" \
  "v_mad_u32_u16 v180, v160, 4, v65 op_sel:[1,0,0,0]\n" \
  "v_max_u32_e32 v180, v180, v70\n" \
  "v_cvt_pk_bf16_f32 v168, v186, v187\n" \
  "v_cvt_pk_bf16_f32 v169, v188, v189\n" \
  "v_mfma_f32_32x32x16_bf16 v[96:111], v[116:119], v[24:27], v[96:111]\n" \
  "ds_read_b128 v[132:135], v175\n" \
  "v_rcp_f32_e32 v162, v162\n" \
  "v_rcp_f32_e32 v163, v163\n" \
  "v_rcp_f32_e32 v164, v164\n" \
  "v_mfma_f32_32x32x16_bf16 v[96:111], v[124:127], v[28:31], v[96:111]\n" \
  "ds_read_b32 v166, v77 offset:256\n" \
  "v_rcp_f32_e32 v165, v165\n" \
  "v_exp_f32_e32 v141, v80\n" \
  "v_exp_f32_e32 v142, v85\n" \
  "v_mfma_f32_32x32x16_bf16 v[96:111], v[128:131], v[36:39], v[96:111]\n" \
  "ds_read_b32 v160, v74 offset:256\n" \
  "v_exp_f32_e32 v143, v90\n" \
  "v_exp_f32_e32 v140, v95\n" \
  "v_exp_f32_e32 v145, v81\n" \
  "ds_read_b32 v161, v75\n" \
  "v_exp_f32_e32 v146, v86\n" \
  "v_exp_f32_e32 v147, v91\n" \
  "v_exp_f32_e32 v144, v92\n" \
  "s_add_u32 m0, %[stg], 4096\n" \
  "v_exp_f32_e32 v149, v82\n" \
  "v_exp_f32_e32 v150, v87\n" \
  "v_exp_f32_e32 v151, v88\n" \
  "v_exp_f32_e32 v148, v93\n" \
  "v_exp_f32_e32 v153, v83\n" \
  "v_exp_f32_e32 v154, v84\n" \
  "v_exp_f32_e32 v155, v89\n" \
  "v_exp_f32_e32 v152, v94\n" \
  "v_min3_f32 v64, v64, v162, v163\n" \
  "v_min3_f32 v64, v64, v164, v165\n" \
  "s_setprio 0\n" \
  "v_mul_f32_e32 v186, v156, v162\n" \
  "v_mul_f32_e32 v187, v157, v163\n" \
  "v_mul_f32_e32 v188, v158, v164\n" \
  "v_mul_f32_e32 v189, v159, v165\n" \
  "v_add_f32_e32 v162, 1.0, v141\n" \
  "v_add_f32_e32 v163, 1.0, v142\n" \
  "v_add_f32_e32 v164, 1.0, v143\n" \
  "v_add_f32_e32 v165, 1.0, v140\n" \
  "v_fma_f32 v156, v80, v145, v80\n" \
  "v_fma_f32 v157, v85, v146, v85\n" \
  "v_fma_f32 v158, v90, v147, v90\n" \
  "v_fma_f32 v159, v95, v144, v95\n" \
  "s_waitcnt vmcnt(0)\n" \
  "ds_read_b128 v[112:115], v66\n" \
  "v_fmac_f32_e32 v156, v81, v162\n" \
  "ds_read_b128 v[116:119], v67\n" \
  "v_fmac_f32_e32 v157, v86, v163\n" \
  "ds_read_b128 v[120:123], v68\n" \
  "v_fmac_f32_e32 v158, v91, v164\n" \
  "ds_read_b128 v[124:127], v69\n" \
  "v_fmac_f32_e32 v159, v92, v165\n" \
  "ds_read_b128 v[128:131], v180\n" \
  "v_fmac_f32_e32 v162, v162, v145\n" \
  "v_fmac_f32_e32 v163, v163, v146\n" \
  "v_fmac_f32_e32 v164, v164, v147\n" \
  "v_fmac_f32_e32 v165, v165, v144\n" \
  "v_fmac_f32_e32 v156, v156, v149\n" \
  "v_fmac_f32_e32 v157, v157, v150\n" \
  "v_fmac_f32_e32 v158, v158, v151\n" \
  "v_fmac_f32_e32 v159, v159, v148\n" \
  "v_fmac_f32_e32 v156, v82, v162\n" \
  "v_fmac_f32_e32 v157, v87, v163\n" \
  "v_fmac_f32_e32 v158, v88, v164\n" \
  "v_fmac_f32_e32 v159, v93, v165\n" \
  "v_fmac_f32_e32 v162, v162, v149\n" \
  "v_fmac_f32_e32 v163, v163, v150\n" \
  "v_fmac_f32_e32 v164, v164, v151\n" \
  "v_fmac_f32_e32 v165, v165, v148\n" \
  "v_fmac_f32_e32 v156, v156, v153\n" \
  "v_fmac_f32_e32 v157, v157, v154\n" \
  "v_fmac_f32_e32 v158, v158, v155\n" \
  "v_fmac_f32_e32 v159, v159, v152\n" \
  "v_fmac_f32_e32 v156, v83, v162\n" \
  "v_fmac_f32_e32 v157, v84, v163\n" \
  "v_fmac_f32_e32 v158, v89, v164\n" \
  "v_fmac_f32_e32 v159, v94, v165\n" \
  "v_fmac_f32_e32 v162, v162, v153\n" \
  "v_fmac_f32_e32 v163, v163, v154\n" \
  "v_fmac_f32_e32 v164, v164, v155\n" \
  "v_fmac_f32_e32 v165, v165, v152\n" \
  "s_setprio 1\n" \
  "s_waitcnt lgkmcnt(0)\n" \
  "v_and_or_b32 v174, v166, %[m32], v73\n" \
  "v_mfma_f32_32x32x16_bf16 v[80:95], v[112:115], v[24:27], 0\n" \
  "buffer_load_dwordx4 v174, %[re], %[np32] offen lds\n" \
  "v_mad_u32_u16 v176, v172, %[c128], v182\n" \
  "v_mad_u32_u16 v177, v172, %[c128], v183 op_sel:[1,0,0,0]\n" \
  "v_cvt_pk_bf16_f32 v170, v186, v187\n" \
  "v_cvt_pk_bf16_f32 v171, v188, v189\n" \
  "s_mov_b32 m0, %[stg]\n" \
  "v_rcp_f32_e32 v162, v162\n" \
  "v_mfma_f32_32x32x16_bf16 v[80:95], v[120:123], v[28:31], v[80:95]\n" \
  "buffer_load_dwordx4 v176, %[rx], 0 offen sc1 lds\n" \
  "v_rcp_f32_e32 v163, v163\n" \
  "v_rcp_f32_e32 v164, v164\n" \
  "v_mad_u32_u16 v178, v173, %[c128], v184\n" \
  "v_mad_u32_u16 v179, v173, %[c128], v185 op_sel:[1,0,0,0]\n" \
  "v_mfma_f32_32x32x16_bf16 v[80:95], v[128:131], v[32:35], v[80:95]\n" \
  "buffer_load_dwordx4 v177, %[rx], 0 offen offset:1024 sc1 lds\n" \
  "v_rcp_f32_e32 v165, v165\n" \
  "v_and_b32_e32 v175, 15, v161\n" \
  "v_mad_u32_u24 v175, v175, %[luts], v72\n" \
  "v_exp_f32_e32 v141, v96\n" \
  "v_mfma_f32_32x32x16_bf16 v[48:63], v[168:171], v[136:139], v[48:63]\n" \
  "buffer_load_dwordx4 v178, %[rx], 0 offen offset:2048 sc1 lds\n" \
  "v_exp_f32_e32 v142, v101\n" \
  "v_exp_f32_e32 v143, v106\n" \
  "v_exp_f32_e32 v140, v111\n" \
  "v_mfma_f32_32x32x16_bf16 v[80:95], v[132:135], v[40:43], v[80:95]\n" \
  "buffer_load_dwordx4 v179, %[rx], 0 offen offset:3072 sc1 lds\n" \
  "v_exp_f32_e32 v145, v97\n" \
  "v_exp_f32_e32 v146, v102\n" \
  "v_exp_f32_e32 v147, v107\n" \
  "ds_read_b64_tr_b16 v[136:137], v175 offset:32\n" \
  "v_exp_f32_e32 v144, v108\n" \
  "v_exp_f32_e32 v149, v98\n" \
  "v_exp_f32_e32 v150, v103\n" \
  "ds_read_b64_tr_b16 v[138:139], v175\n" \
  "v_exp_f32_e32 v151, v104\n" \
  "v_exp_f32_e32 v148, v109\n" \
  "v_exp_f32_e32 v153, v99\n" \
  "v_exp_f32_e32 v154, v100\n" \
  "v_exp_f32_e32 v155, v105\n" \
  "v_exp_f32_e32 v152, v110\n" \
  "v_min3_f32 v64, v64, v162, v163\n" \
  "v_min3_f32 v64, v64, v164, v165\n" \
  "s_setprio 0\n" \
  "v_mul_f32_e32 v186, v156, v162\n" \
  "v_mul_f32_e32 v187, v157, v163\n" \
  "v_mul_f32_e32 v188, v158, v164\n" \
  "v_mul_f32_e32 v189, v159, v165\n" \
  "v_add_f32_e32 v162, 1.0, v141\n" \
  "v_add_f32_e32 v163, 1.0, v142\n" \
  "v_add_f32_e32 v164, 1.0, v143\n" \
  "v_add_f32_e32 v165, 1.0, v140\n" \
  "v_fma_f32 v156, v96, v145, v96\n" \
  "v_fma_f32 v157, v101, v146, v101\n" \
  "v_fma_f32 v158, v106, v147, v106\n" \
  "v_fma_f32 v159, v111, v144, v111\n" \
  "v_fmac_f32_e32 v156, v97, v162\n" \
  "v_fmac_f32_e32 v157, v102, v163\n" \
  "v_fmac_f32_e32 v158, v107, v164\n" \
  "v_fmac_f32_e32 v159, v108, v165\n" \
  "v_fmac_f32_e32 v162, v162, v145\n" \
  "v_fmac_f32_e32 v163, v163, v146\n" \
  "v_fmac_f32_e32 v164, v164, v147\n" \
  "v_fmac_f32_e32 v165, v165, v144\n" \
  "v_fmac_f32_e32 v156, v156, v149\n" \
  "v_fmac_f32_e32 v157, v157, v150\n" \
  "v_fmac_f32_e32 v158, v158, v151\n" \
  "v_fmac_f32_e32 v159, v159, v148\n" \
  "v_fmac_f32_e32 v156, v98, v162\n" \
  "v_fmac_f32_e32 v157, v103, v163\n" \
  "v_fmac_f32_e32 v158, v104, v164\n" \
  "v_fmac_f32_e32 v159, v109, v165\n" \
  "v_fmac_f32_e32 v162, v162, v149\n" \
  "v_fmac_f32_e32 v163, v163, v150\n" \
  "v_fmac_f32_e32 v164, v164, v151\n" \
  "v_fmac_f32_e32 v165, v165, v148\n" \
  "v_fmac_f32_e32 v156, v156, v153\n" \
  "v_fmac_f32_e32 v157, v157, v154\n" \
  "v_fmac_f32_e32 v158, v158, v155\n" \
  "v_fmac_f32_e32 v159, v159, v152\n" \
  "v_fmac_f32_e32 v156, v99, v162\n" \
  "v_fmac_f32_e32 v157, v100, v163\n" \
  "v_fmac_f32_e32 v158, v105, v164\n" \
  "v_fmac_f32_e32 v159, v110, v165\n" \
  "v_fmac_f32_e32 v162, v162, v153\n" \
  "v_fmac_f32_e32 v163, v163, v154\n" \
  "v_fmac_f32_e32 v164, v164, v155\n" \
  "v_fmac_f32_e32 v165, v165, v152\n" \
  "s_sub_u32 %[rem], %[rem], 1\n" \
  "s_cbranch_scc1 END_%=\n" \
  "s_setprio 1\n" \
  "v_mfma_f32_32x32x16_bf16 v[96:111], v[132:135], v[44:47], 0\n" \
  "ds_read_b64 v[172:173], v76 offset:192\n" \
  "v_and_b32_e32 v175, 15, v160\n" \
  "v_mad_u32_u24 v175, v175, %[luts], v71\n" \
  "v_mad_u32_u16 v180, v160, 4, v65 op_sel:[1,0,0,0]\n" \
  "v_max_u32_e32 v180, v180, v70\n" \
  "v_cvt_pk_bf16_f32 v168, v186, v187\n" \
  "v_cvt_pk_bf16_f32 v169, v188, v189\n" \
  "v_mfma_f32_32x32x16_bf16 v[96:111], v[116:119], v[24:27], v[96:111]\n" \
  "ds_read_b128 v[132:135], v175\n" \
  "v_rcp_f32_e32 v162, v162\n" \
  "v_rcp_f32_e32 v163, v163\n" \
  "v_rcp_f32_e32 v164, v164\n" \
  "v_mfma_f32_32x32x16_bf16 v[96:111], v[124:127], v[28:31], v[96:111]\n" \
  "ds_read_b32 v166, v77 offset:384\n" \
  "v_rcp_f32_e32 v165, v165\n" \
  "v_exp_f32_e32 v141, v80\n" \
  "v_exp_f32_e32 v142, v85\n" \
  "v_mfma_f32_32x32x16_bf16 v[96:111], v[128:131], v[36:39], v[96:111]\n" \
  "ds_read_b32 v160, v74 offset:384\n" \
  "v_exp_f32_e32 v143, v90\n" \
  "v_exp_f32_e32 v140, v95\n" \
  "v_exp_f32_e32 v145, v81\n" \
  "ds_read_b32 v161, v75 offset:128\n" \
  "v_exp_f32_e32 v146, v86\n" \
  "v_exp_f32_e32 v147, v91\n" \
  "v_exp_f32_e32 v144, v92\n" \
  "s_add_u32 m0, %[stg], 4096\n" \
  "v_exp_f32_e32 v149, v82\n" \
  "v_exp_f32_e32 v150, v87\n" \
  "v_exp_f32_e32 v151, v88\n" \
  "v_exp_f32_e32 v148, v93\n" \
  "v_exp_f32_e32 v153, v83\n" \
  "v_exp_f32_e32 v154, v84\n" \
  "v_exp_f32_e32 v155, v89\n" \
  "v_exp_f32_e32 v152, v94\n" \
  "v_min3_f32 v64, v64, v162, v163\n" \
  "v_min3_f32 v64, v64, v164, v165\n" \
  "s_setprio 0\n" \
  "v_mul_f32_e32 v186, v156, v162\n" \
  "v_mul_f32_e32 v187, v157, v163\n" \
  "v_mul_f32_e32 v188, v158, v164\n" \
  "v_mul_f32_e32 v189, v159, v165\n" \
  "v_add_f32_e32 v162, 1.0, v141\n" \
  "v_add_f32_e32 v163, 1.0, v142\n" \
  "v_add_f32_e32 v164, 1.0, v143\n" \
  "v_add_f32_e32 v165, 1.0, v140\n" \
  "v_fma_f32 v156, v80, v145, v80\n" \
  "v_fma_f32 v157, v85, v146, v85\n" \
  "v_fma_f32 v158, v90, v147, v90\n" \
  "v_fma_f32 v159, v95, v144, v95\n" \
  "s_waitcnt vmcnt(0)\n" \
  "ds_read_b128 v[112:115], v66\n" \
  "v_fmac_f32_e32 v156, v81, v162\n" \
  "ds_read_b128 v[116:119], v67\n" \
  "v_fmac_f32_e32 v157, v86, v163\n" \
  "ds_read_b128 v[120:123], v68\n" \
  "v_fmac_f32_e32 v158, v91, v164\n" \
  "ds_read_b128 v[124:127], v69\n" \
  "v_fmac_f32_e32 v159, v92, v165\n" \
  "ds_read_b128 v[128:131], v180\n" \
  "v_fmac_f32_e32 v162, v162, v145\n" \
  "v_fmac_f32_e32 v163, v163, v146\n" \
  "v_fmac_f32_e32 v164, v164, v147\n" \
  "v_fmac_f32_e32 v165, v165, v144\n" \
  "v_fmac_f32_e32 v156, v156, v149\n" \
  "v_fmac_f32_e32 v157, v157, v150\n" \
  "v_fmac_f32_e32 v158, v158, v151\n" \
  "v_fmac_f32_e32 v159, v159, v148\n" \
  "v_fmac_f32_e32 v156, v82, v162\n" \
  "v_fmac_f32_e32 v157, v87, v163\n" \
  "v_fmac_f32_e32 v158, v88, v164\n" \
  "v_fmac_f32_e32 v159, v93, v165\n" \
  "v_fmac_f32_e32 v162, v162, v149\n" \
  "v_fmac_f32_e32 v163, v163, v150\n" \
  "v_fmac_f32_e32 v164, v164, v151\n" \
  "v_fmac_f32_e32 v165, v165, v148\n" \
  "v_fmac_f32_e32 v156, v156, v153\n" \
  "v_fmac_f32_e32 v157, v157, v154\n" \
  "v_fmac_f32_e32 v158, v158, v155\n" \
  "v_fmac_f32_e32 v159, v159, v152\n" \
  "v_fmac_f32_e32 v156, v83, v162\n" \
  "v_fmac_f32_e32 v157, v84, v163\n" \
  "v_fmac_f32_e32 v158, v89, v164\n" \
  "v_fmac_f32_e32 v159, v94, v165\n" \
  "v_fmac_f32_e32 v162, v162, v153\n" \
  "v_fmac_f32_e32 v163, v163, v154\n" \
  "v_fmac_f32_e32 v164, v164, v155\n" \
  "v_fmac_f32_e32 v165, v165, v152\n" \
  "s_setprio 1\n" \
  "s_waitcnt lgkmcnt(0)\n" \
  "v_and_or_b32 v174, v166, %[m32], v73\n" \
  "v_mfma_f32_32x32x16_bf16 v[80:95], v[112:115], v[24:27], 0\n" \
  "buffer_load_dwordx4 v174, %[re], %[np32] offen lds\n" \
  "v_mad_u32_u16 v176, v172, %[c128], v182\n" \
  "v_mad_u32_u16 v177, v172, %[c128], v183 op_sel:[1,0,0,0]\n" \
  "v_cvt_pk_bf16_f32 v170, v186, v187\n" \
  "v_cvt_pk_bf16_f32 v171, v188, v189\n" \
  "s_mov_b32 m0, %[stg]\n" \
  "v_rcp_f32_e32 v162, v162\n" \
  "v_mfma_f32_32x32x16_bf16 v[80:95], v[120:123], v[28:31], v[80:95]\n" \
  "buffer_load_dwordx4 v176, %[rx], 0 offen sc1 lds\n" \
  "v_rcp_f32_e32 v163, v163\n" \
  "v_rcp_f32_e32 v164, v164\n" \
  "v_mad_u32_u16 v178, v173, %[c128], v184\n" \
  "v_mad_u32_u16 v179, v173, %[c128], v185 op_sel:[1,0,0,0]\n" \
  "v_mfma_f32_32x32x16_bf16 v[80:95], v[128:131], v[32:35], v[80:95]\n" \
  "buffer_load_dwordx4 v177, %[rx], 0 offen offset:1024 sc1 lds\n" \
  "v_rcp_f32_e32 v165, v165\n" \
  "v_and_b32_e32 v175, 15, v161\n" \
  "v_mad_u32_u24 v175, v175, %[luts], v72\n" \
  "v_exp_f32_e32 v141, v96\n" \
  "v_mfma_f32_32x32x16_bf16 v[48:63], v[168:171], v[136:139], v[48:63]\n" \
  "buffer_load_dwordx4 v178, %[rx], 0 offen offset:2048 sc1 lds\n" \
  "v_exp_f32_e32 v142, v101\n" \
  "v_exp_f32_e32 v143, v106\n" \
  "v_exp_f32_e32 v140, v111\n" \
  "v_mfma_f32_32x32x16_bf16 v[80:95], v[132:135], v[40:43], v[80:95]\n" \
  "buffer_load_dwordx4 v179, %[rx], 0 offen offset:3072 sc1 lds\n" \
  "v_exp_f32_e32 v145, v97\n" \
  "v_exp_f32_e32 v146, v102\n" \
  "v_exp_f32_e32 v147, v107\n" \
  "ds_read_b64_tr_b16 v[136:137], v175 offset:32\n" \
  "v_exp_f32_e32 v144, v108\n" \
  "v_exp_f32_e32 v149, v98\n" \
  "v_exp_f32_e32 v150, v103\n" \
  "ds_read_b64_tr_b16 v[138:139], v175\n" \
  "v_exp_f32_e32 v151, v104\n" \
  "v_exp_f32_e32 v148, v109\n" \
  "v_exp_f32_e32 v153, v99\n" \
  "v_exp_f32_e32 v154, v100\n" \
  "v_exp_f32_e32 v155, v105\n" \
  "v_exp_f32_e32 v152, v110\n" \
  "v_min3_f32 v64, v64, v162, v163\n" \
  "v_min3_f32 v64, v64, v164, v165\n" \
  "s_setprio 0\n" \
  "v_mul_f32_e32 v186, v156, v162\n" \
  "v_mul_f32_e32 v187, v157, v163\n" \
  "v_mul_f32_e32 v188, v158, v164\n" \
  "v_mul_f32_e32 v189, v159, v165\n" \
  "v_add_f32_e32 v162, 1.0, v141\n" \
  "v_add_f32_e32 v163, 1.0, v142\n" \
  "v_add_f32_e32 v164, 1.0, v143\n" \
  "v_add_f32_e32 v165, 1.0, v140\n" \
  "v_fma_f32 v156, v96, v145, v96\n" \
  "v_fma_f32 v157, v101, v146, v101\n" \
  "v_fma_f32 v158, v106, v147, v106\n" \
  "v_fma_f32 v159, v111, v144, v111\n" \
  "v_fmac_f32_e32 v156, v97, v162\n" \
  "v_fmac_f32_e32 v157, v102, v163\n" \
  "v_fmac_f32_e32 v158, v107, v164\n" \
  "v_fmac_f32_e32 v159, v108, v165\n" \
  "v_fmac_f32_e32 v162, v162, v145\n" \
  "v_fmac_f32_e32 v163, v163, v146\n" \
  "v_fmac_f32_e32 v164, v164, v147\n" \
  "v_fmac_f32_e32 v165, v165, v144\n" \
  "v_fmac_f32_e32 v156, v156, v149\n" \
  "v_fmac_f32_e32 v157, v157, v150\n" \
  "v_fmac_f32_e32 v158, v158, v151\n" \
  "v_fmac_f32_e32 v159, v159, v148\n" \
  "v_fmac_f32_e32 v156, v98, v162\n" \
  "v_fmac_f32_e32 v157, v103, v163\n" \
  "v_fmac_f32_e32 v158, v104, v164\n" \
  "v_fmac_f32_e32 v159, v109, v165\n" \
  "v_fmac_f32_e32 v162, v162, v149\n" \
  "v_fmac_f32_e32 v163, v163, v150\n" \
  "v_fmac_f32_e32 v164, v164, v151\n" \
  "v_fmac_f32_e32 v165, v165, v148\n" \
  "v_fmac_f32_e32 v156, v156, v153\n" \
  "v_fmac_f32_e32 v157, v157, v154\n" \
  "v_fmac_f32_e32 v158, v158, v155\n" \
  "v_fmac_f32_e32 v159, v159, v152\n" \
  "v_fmac_f32_e32 v156, v99, v162\n" \
  "v_fmac_f32_e32 v157, v100, v163\n" \
  "v_fmac_f32_e32 v158, v105, v164\n" \
  "v_fmac_f32_e32 v159, v110, v165\n" \
  "v_fmac_f32_e32 v162, v162, v153\n" \
  "v_fmac_f32_e32 v163, v163, v154\n" \
  "v_fmac_f32_e32 v164, v164, v155\n" \
  "v_fmac_f32_e32 v165, v165, v152\n" \
  "v_add_u32_e32 v74, 256, v74\n" \
  "v_add_u32_e32 v75, 256, v75\n" \
  "v_add_u32_e32 v76, 128, v76\n" \
  "v_add_u32_e32 v77, 256, v77\n" \
  "s_sub_u32 %[rem], %[rem], 1\n" \
  "s_cbranch_scc0 LOOP_%=\n" \
  "END_%=:\n" \
  "v_xor_b32_e32 v174, 64, v78\n" \
  "ds_read_b64_tr_b16 v[96:97], v78\n" \
  "ds_read_b64_tr_b16 v[98:99], v78 offset:1024\n" \
  "ds_read_b64_tr_b16 v[112:113], v174\n" \
  "ds_read_b64_tr_b16 v[114:115], v174 offset:1024\n" \
  "ds_read_b64_tr_b16 v[104:105], v78 offset:4096\n" \
  "ds_read_b64_tr_b16 v[106:107], v78 offset:5120\n" \
  "ds_read_b64_tr_b16 v[120:121], v174 offset:4096\n" \
  "ds_read_b64_tr_b16 v[122:123], v174 offset:5120\n" \
  "ds_read_b64_tr_b16 v[100:101], v78 offset:2048\n" \
  "ds_read_b64_tr_b16 v[102:103], v78 offset:3072\n" \
  "ds_read_b64_tr_b16 v[116:117], v174 offset:2048\n" \
  "ds_read_b64_tr_b16 v[118:119], v174 offset:3072\n" \
  "v_cvt_pk_bf16_f32 v168, v186, v187\n" \
  "v_cvt_pk_bf16_f32 v169, v188, v189\n" \
  "v_rcp_f32_e32 v162, v162\n" \
  "v_rcp_f32_e32 v163, v163\n" \
  "v_rcp_f32_e32 v164, v164\n" \
  "v_rcp_f32_e32 v165, v165\n" \
  "s_nop 0\n" \
  "v_mul_f32_e32 v186, v156, v162\n" \
  "v_mul_f32_e32 v187, v157, v163\n" \
  "v_mul_f32_e32 v188, v158, v164\n" \
  "v_mul_f32_e32 v189, v159, v165\n" \
  "v_cvt_pk_bf16_f32 v170, v186, v187\n" \
  "v_cvt_pk_bf16_f32 v171, v188, v189\n" \
  "v_min3_f32 v64, v64, v162, v163\n" \
  "v_min3_f32 v64, v64, v164, v165\n" \
  "s_waitcnt lgkmcnt(12)\n" \
  "v_mfma_f32_32x32x16_bf16 v[48:63], v[168:171], v[136:139], v[48:63]\n" \
  "ds_read_b64_tr_b16 v[108:109], v78 offset:6144\n" \
  "ds_read_b64_tr_b16 v[110:111], v78 offset:7168\n" \
  "ds_read_b64_tr_b16 v[124:125], v174 offset:6144\n" \
  "ds_read_b64_tr_b16 v[126:127], v174 offset:7168\n" \
  "v_mov_b32_e32 v136, 0\n" \
  "v_mov_b32_e32 v144, 0\n" \
  "v_mov_b32_e32 v137, 0\n" \
  "v_mov_b32_e32 v145, 0\n" \
  "v_mov_b32_e32 v138, 0\n" \
  "v_mov_b32_e32 v146, 0\n" \
  "v_mov_b32_e32 v139, 0\n" \
  "v_mov_b32_e32 v147, 0\n" \
  "v_mov_b32_e32 v140, 0\n" \
  "v_mov_b32_e32 v148, 0\n" \
  "v_mov_b32_e32 v141, 0\n" \
  "v_mov_b32_e32 v149, 0\n" \
  "v_mov_b32_e32 v142, 0\n" \
  "v_mov_b32_e32 v150, 0\n" \
  "v_mov_b32_e32 v143, 0\n" \
  "v_mov_b32_e32 v151, 0\n" \
  "s_nop 1\n" \
  "v_cvt_pk_bf16_f32 v128, v48, v49\n" \
  "v_cvt_pk_bf16_f32 v129, v50, v51\n" \
  "v_cvt_pk_bf16_f32 v130, v52, v53\n" \
  "v_cvt_pk_bf16_f32 v131, v54, v55\n" \
  "v_mov_b32_e32 v152, v128\n" \
  "v_mov_b32_e32 v153, v129\n" \
  "v_mov_b32_e32 v154, v130\n" \
  "v_mov_b32_e32 v155, v131\n" \
  "v_permlane16_swap_b32_e32 v128, v136\n" \
  "v_permlane16_swap_b32_e32 v129, v137\n" \
  "v_permlane16_swap_b32_e32 v130, v138\n" \
  "v_permlane16_swap_b32_e32 v131, v139\n" \
  "v_permlane16_swap_b32_e32 v144, v152\n" \
  "v_permlane16_swap_b32_e32 v145, v153\n" \
  "v_permlane16_swap_b32_e32 v146, v154\n" \
  "v_permlane16_swap_b32_e32 v147, v155\n" \
  "s_waitcnt lgkmcnt(0)\n" \
  "v_mfma_f32_32x32x16_bf16 v[190:205], v[96:99], v[128:131], v[222:237]\n" \
  "v_mfma_f32_32x32x16_bf16 v[190:205], v[104:107], v[136:139], v[190:205]\n" \
  "v_mfma_f32_32x32x16_bf16 v[190:205], v[112:115], v[144:147], v[190:205]\n" \
  "v_mfma_f32_32x32x16_bf16 v[190:205], v[120:123], v[152:155], v[190:205]\n" \
  "v_cvt_pk_bf16_f32 v132, v56, v57\n" \
  "v_cvt_pk_bf16_f32 v133, v58, v59\n" \
  "v_cvt_pk_bf16_f32 v134, v60, v61\n" \
  "v_cvt_pk_bf16_f32 v135, v62, v63\n" \
  "v_mov_b32_e32 v156, v132\n" \
  "v_mov_b32_e32 v157, v133\n" \
  "v_mov_b32_e32 v158, v134\n" \
  "v_mov_b32_e32 v159, v135\n" \
  "v_permlane16_swap_b32_e32 v132, v140\n" \
  "v_permlane16_swap_b32_e32 v133, v141\n" \
  "v_permlane16_swap_b32_e32 v134, v142\n" \
  "v_permlane16_swap_b32_e32 v135, v143\n" \
  "v_permlane16_swap_b32_e32 v148, v156\n" \
  "v_permlane16_swap_b32_e32 v149, v157\n" \
  "v_permlane16_swap_b32_e32 v150, v158\n" \
  "v_permlane16_swap_b32_e32 v151, v159\n" \
  "v_mfma_f32_32x32x16_bf16 v[190:205], v[100:103], v[132:135], v[190:205]\n" \
  "v_mfma_f32_32x32x16_bf16 v[190:205], v[108:111], v[140:143], v[190:205]\n" \
  "v_mfma_f32_32x32x16_bf16 v[190:205], v[116:119], v[148:151], v[190:205]\n" \
  "v_mfma_f32_32x32x16_bf16 v[190:205], v[124:127], v[156:159], v[190:205]\n" \
  "s_waitcnt vmcnt(0)\n" \
  "s_nop 11\n"
#define QLOOP_CLOB "v48", "v49", "v50", "v51", "v52", "v53", "v54", "v55", "v56", "v57", "v58", "v59", "v60", "v61", "v62", "v63", "v80", "v81", "v82", "v83", "v84", "v85", "v86", "v87", "v88", "v89", "v90", "v91", "v92", "v93", "v94", "v95", "v96", "v97", "v98", "v99", "v100", "v101", "v102", "v103", "v104", "v105", "v106", "v107", "v108", "v109", "v110", "v111", "v112", "v113", "v114", "v115", "v116", "v117", "v118", "v119", "v120", "v121", "v122", "v123", "v124", "v125", "v126", "v127", "v128", "v129", "v130", "v131", "v132", "v133", "v134", "v135", "v136", "v137", "v138", "v139", "v140", "v141", "v142", "v143", "v144", "v145", "v146", "v147", "v148", "v149", "v150", "v151", "v152", "v153", "v154", "v155", "v156", "v157", "v158", "v159", "v160", "v161", "v162", "v163", "v164", "v165", "v166", "v167", "v168", "v169", "v170", "v171", "v172", "v173", "v174", "v175", "v176", "v177", "v178", "v179", "v180", "v181", "v186", "v187", "v188", "v189"

#define FRONT_ASM \
  "v_mov_b32_e32 v180, 4\n" \
  "v_cmp_gt_u32_e64 s[60:61], %[nr4], v102\n" \
  "v_or_b32_e32 v168, 1, v102\n" \
  "v_cmp_gt_u32_e64 s[62:63], %[nr4], v168\n" \
  "s_add_u32 m0, %[wv1k], 117120\n" \
  "s_add_u32 s68, %[eo], 8192\n" \
  "buffer_load_dwordx4 v88, %[re], s68 offen lds\n" \
  "s_add_u32 m0, %[wv1k], 125312\n" \
  "s_add_u32 s68, %[eo], 16384\n" \
  "buffer_load_dwordx4 v88, %[re], s68 offen lds\n" \
  "s_cmp_eq_u32 %[wv1k], 7168\n" \
  "s_cbranch_scc1 W7A_%=\n" \
  "s_waitcnt vmcnt(3)\n" \
  "v_mul_u32_u24_e32 v0, %[ca], v0\n" \
  "v_add_lshl_u32 v168, v8, %[xb], 17\n" \
  "v_bfi_b32 v112, %[m80], v168, v0\n" \
  "v_lshrrev_b32_e32 v112, 20, v112\n" \
  "v_and_b32_e32 v112, 0xff8, v112\n" \
  "ds_add_rtn_u32 v120, v112, v180 offset:36864\n" \
  "v_mul_u32_u24_e32 v1, %[ca], v1\n" \
  "v_add_lshl_u32 v168, v9, %[xb], 17\n" \
  "v_bfi_b32 v113, %[m80], v168, v1\n" \
  "v_lshrrev_b32_e32 v113, 20, v113\n" \
  "v_and_b32_e32 v113, 0xff8, v113\n" \
  "ds_add_rtn_u32 v121, v113, v180 offset:36864\n" \
  "v_mul_u32_u24_e32 v2, %[ca], v2\n" \
  "v_add_lshl_u32 v168, v10, %[xb], 17\n" \
  "v_bfi_b32 v114, %[m80], v168, v2\n" \
  "v_lshrrev_b32_e32 v114, 20, v114\n" \
  "v_and_b32_e32 v114, 0xff8, v114\n" \
  "ds_add_rtn_u32 v122, v114, v180 offset:36864\n" \
  "v_mul_u32_u24_e32 v4, %[ca], v4\n" \
  "v_add_lshl_u32 v168, v12, %[xb], 17\n" \
  "v_bfi_b32 v115, %[m80], v168, v4\n" \
  "v_lshrrev_b32_e32 v115, 20, v115\n" \
  "v_and_b32_e32 v115, 0xff8, v115\n" \
  "s_mov_b64 exec, s[60:61]\n" \
  "ds_add_rtn_u32 v123, v115, v180 offset:36868\n" \
  "v_mul_u32_u24_e32 v5, %[ca], v5\n" \
  "v_add_lshl_u32 v168, v13, %[xb], 17\n" \
  "v_bfi_b32 v116, %[m80], v168, v5\n" \
  "v_lshrrev_b32_e32 v116, 20, v116\n" \
  "v_and_b32_e32 v116, 0xff8, v116\n" \
  "s_mov_b64 exec, s[62:63]\n" \
  "ds_add_rtn_u32 v124, v116, v180 offset:36868\n" \
  "v_mul_u32_u24_e32 v6, %[ca], v6\n" \
  "v_add_lshl_u32 v168, v14, %[xb], 17\n" \
  "v_bfi_b32 v117, %[m80], v168, v6\n" \
  "v_lshrrev_b32_e32 v117, 20, v117\n" \
  "v_and_b32_e32 v117, 0xff8, v117\n" \
  "ds_add_rtn_u32 v125, v117, v180 offset:36868\n" \
  "v_mul_u32_u24_e32 v7, %[ca], v7\n" \
  "v_add_lshl_u32 v168, v15, %[xb], 17\n" \
  "v_bfi_b32 v118, %[m80], v168, v7\n" \
  "v_lshrrev_b32_e32 v118, 20, v118\n" \
  "v_and_b32_e32 v118, 0xff8, v118\n" \
  "ds_add_rtn_u32 v126, v118, v180 offset:36868\n" \
  "s_mov_b64 exec, -1\n" \
  "v_add_lshl_u32 v8, v8, %[xb], 16\n" \
  "v_lshlrev_b32_e32 v168, 9, v0\n" \
  "v_bfe_u32 v0, v0, 16, 5\n" \
  "v_bfi_b32 v8, %[mc0], v168, v8\n" \
  "v_or_b32_e32 v128, v8, v0\n" \
  "v_add_lshl_u32 v9, v9, %[xb], 16\n" \
  "v_lshlrev_b32_e32 v168, 9, v1\n" \
  "v_bfe_u32 v1, v1, 16, 5\n" \
  "v_bfi_b32 v9, %[mc0], v168, v9\n" \
  "v_or_b32_e32 v129, v9, v1\n" \
  "v_add_lshl_u32 v10, v10, %[xb], 16\n" \
  "v_lshlrev_b32_e32 v168, 9, v2\n" \
  "v_bfe_u32 v2, v2, 16, 5\n" \
  "v_bfi_b32 v10, %[mc0], v168, v10\n" \
  "v_or_b32_e32 v130, v10, v2\n" \
  "v_add_lshl_u32 v12, v12, %[xb], 16\n" \
  "v_lshlrev_b32_e32 v168, 9, v4\n" \
  "v_bfe_u32 v4, v4, 16, 5\n" \
  "v_bfi_b32 v12, %[mc0], v168, v12\n" \
  "v_or3_b32 v131, v12, v4, v103\n" \
  "v_add_u32_e32 v103, 32, v103\n" \
  "v_add_lshl_u32 v13, v13, %[xb], 16\n" \
  "v_lshlrev_b32_e32 v168, 9, v5\n" \
  "v_bfe_u32 v5, v5, 16, 5\n" \
  "v_bfi_b32 v13, %[mc0], v168, v13\n" \
  "v_or3_b32 v132, v13, v5, v103\n" \
  "v_add_u32_e32 v103, 32, v103\n" \
  "v_add_lshl_u32 v14, v14, %[xb], 16\n" \
  "v_lshlrev_b32_e32 v168, 9, v6\n" \
  "v_bfe_u32 v6, v6, 16, 5\n" \
  "v_bfi_b32 v14, %[mc0], v168, v14\n" \
  "v_or3_b32 v133, v14, v6, v103\n" \
  "v_add_u32_e32 v103, 32, v103\n" \
  "v_add_lshl_u32 v15, v15, %[xb], 16\n" \
  "v_lshlrev_b32_e32 v168, 9, v7\n" \
  "v_bfe_u32 v7, v7, 16, 5\n" \
  "v_bfi_b32 v15, %[mc0], v168, v15\n" \
  "v_or3_b32 v134, v15, v7, v103\n" \
  "s_branch W7B_%=\n" \
  "W7A_%=:\n" \
  "s_mov_b32 m0, 149888\n" \
  "s_add_u32 s68, %[eo], 33792\n" \
  "buffer_load_dwordx4 v88, %[re], s68 offen lds\n" \
  "s_mov_b32 m0, 150912\n" \
  "s_add_u32 s68, %[eo], 34816\n" \
  "buffer_load_dwordx4 v88, %[re], s68 offen lds\n" \
  "s_mov_b32 m0, 90496\n" \
  "s_movk_i32 s68, 0\n" \
  "buffer_load_dwordx4 v240, %[rm], s68 offen lds\n" \
  "buffer_load_dwordx4 v240, %[rm], s68 offen offset:1024 lds\n" \
  "buffer_load_dwordx4 v240, %[rm], s68 offen offset:2048 lds\n" \
  "buffer_load_dwordx4 v240, %[rm], s68 offen offset:3072 lds\n" \
  "s_mov_b32 m0, 94592\n" \
  "s_movk_i32 s68, 4096\n" \
  "buffer_load_dwordx4 v240, %[rm], s68 offen lds\n" \
  "buffer_load_dwordx4 v240, %[rm], s68 offen offset:1024 lds\n" \
  "buffer_load_dwordx4 v240, %[rm], s68 offen offset:2048 lds\n" \
  "buffer_load_dwordx4 v240, %[rm], s68 offen offset:3072 lds\n" \
  "s_mov_b32 m0, 98688\n" \
  "s_movk_i32 s68, 8192\n" \
  "buffer_load_dwordx4 v240, %[rm], s68 offen lds\n" \
  "buffer_load_dwordx4 v240, %[rm], s68 offen offset:1024 lds\n" \
  "buffer_load_dwordx4 v240, %[rm], s68 offen offset:2048 lds\n" \
  "buffer_load_dwordx4 v240, %[rm], s68 offen offset:3072 lds\n" \
  "s_mov_b32 m0, 102784\n" \
  "s_movk_i32 s68, 12288\n" \
  "buffer_load_dwordx4 v240, %[rm], s68 offen lds\n" \
  "buffer_load_dwordx4 v240, %[rm], s68 offen offset:1024 lds\n" \
  "buffer_load_dwordx4 v240, %[rm], s68 offen offset:2048 lds\n" \
  "buffer_load_dwordx4 v240, %[rm], s68 offen offset:3072 lds\n" \
  "s_mov_b32 m0, 106880\n" \
  "s_movk_i32 s68, 16384\n" \
  "buffer_load_dwordx4 v240, %[rm], s68 offen lds\n" \
  "buffer_load_dwordx4 v240, %[rm], s68 offen offset:1024 lds\n" \
  "s_mov_b32 m0, 74112\n" \
  "s_movk_i32 s68, 0\n" \
  "buffer_load_dwordx4 v240, %[ru], s68 offen lds\n" \
  "buffer_load_dwordx4 v240, %[ru], s68 offen offset:1024 lds\n" \
  "buffer_load_dwordx4 v240, %[ru], s68 offen offset:2048 lds\n" \
  "buffer_load_dwordx4 v240, %[ru], s68 offen offset:3072 lds\n" \
  "s_mov_b32 m0, 78208\n" \
  "s_movk_i32 s68, 4096\n" \
  "buffer_load_dwordx4 v240, %[ru], s68 offen lds\n" \
  "buffer_load_dwordx4 v240, %[ru], s68 offen offset:1024 lds\n" \
  "buffer_load_dwordx4 v240, %[ru], s68 offen offset:2048 lds\n" \
  "buffer_load_dwordx4 v240, %[ru], s68 offen offset:3072 lds\n" \
  "s_mov_b32 m0, 82304\n" \
  "s_movk_i32 s68, 8192\n" \
  "buffer_load_dwordx4 v240, %[ru], s68 offen lds\n" \
  "buffer_load_dwordx4 v240, %[ru], s68 offen offset:1024 lds\n" \
  "buffer_load_dwordx4 v240, %[ru], s68 offen offset:2048 lds\n" \
  "buffer_load_dwordx4 v240, %[ru], s68 offen offset:3072 lds\n" \
  "s_mov_b32 m0, 86400\n" \
  "s_movk_i32 s68, 12288\n" \
  "buffer_load_dwordx4 v240, %[ru], s68 offen lds\n" \
  "buffer_load_dwordx4 v240, %[ru], s68 offen offset:1024 lds\n" \
  "buffer_load_dwordx4 v240, %[ru], s68 offen offset:2048 lds\n" \
  "buffer_load_dwordx4 v240, %[ru], s68 offen offset:3072 lds\n" \
  "s_waitcnt vmcnt(36)\n" \
  "W7B_%=:\n" \
  "v_readfirstlane_b32 s64, v16\n" \
  "v_readfirstlane_b32 s65, v17\n" \
  "s_lshl_b32 s68, %[wv1k], 1\n" \
  "s_add_u32 m0, s68, 20480\n" \
  "s_nop 0\n" \
  "buffer_load_dwordx4 v238, %[rxx], 0 offen lds\n" \
  "buffer_load_dwordx4 v239, %[rxx], 0 offen offset:1024 lds\n" \
  "s_add_u32 m0, %[wv1k], 133504\n" \
  "s_add_u32 s68, %[eo], 24576\n" \
  "buffer_load_dwordx4 v88, %[re], s68 offen lds\n" \
  "s_add_u32 m0, %[wv1k], 141696\n" \
  "s_add_u32 s68, %[eo], 32768\n" \
  "buffer_load_dwordx4 v88, %[re], s68 offen lds\n" \
  "s_mul_i32 s78, s64, 0x7f4a7c15\n" \
  "s_mul_hi_u32 s79, s64, 0x7f4a7c15\n" \
  "s_mul_i32 s64, s64, 0x9e3779b9\n" \
  "s_mul_i32 s65, s65, 0x7f4a7c15\n" \
  "s_add_u32 s79, s79, s64\n" \
  "s_add_u32 s79, s79, s65\n" \
  "s_add_u32 s78, s78, 0xd192ed03\n" \
  "s_addc_u32 s79, s79, 0xd1b54a32\n" \
  "s_waitcnt lgkmcnt(0)\n" \
  "s_barrier\n" \
  "ds_read2st64_b32 v[168:169], v102 offset0:144 offset1:152\n" \
  "s_waitcnt lgkmcnt(0)\n" \
  "v_add_u32_e32 v170, v168, v169\n" \
  "v_mul_u32_u24_e32 v170, v107, v170\n" \
  "v_mov_b32_e32 v171, v170\n" \
  "s_lshr_b32 s68, %[wv1k], 8\n" \
  "s_add_u32 s68, s68, 71712\n" \
  "v_add_u32_dpp v171, v171, v171 row_shr:1 row_mask:0xf bank_mask:0xf bound_ctrl:1\n" \
  "v_mov_b32_e32 v172, s68\n" \
  "v_subrev_u32_e32 v170, %[sh], v170\n" \
  "v_add_u32_dpp v171, v171, v171 row_shr:2 row_mask:0xf bank_mask:0xf bound_ctrl:1\n" \
  "s_nop 1\n" \
  "v_add_u32_dpp v171, v171, v171 row_shr:4 row_mask:0xf bank_mask:0xf bound_ctrl:1\n" \
  "s_nop 1\n" \
  "v_add_u32_dpp v171, v171, v171 row_shr:8 row_mask:0xf bank_mask:0xf bound_ctrl:1\n" \
  "s_nop 1\n" \
  "v_add_u32_dpp v171, v171, v171 row_bcast:15 row_mask:0xa bank_mask:0xf\n" \
  "s_nop 1\n" \
  "v_add_u32_dpp v171, v171, v171 row_bcast:31 row_mask:0xc bank_mask:0xf\n" \
  "s_nop 1\n" \
  "s_mov_b32 exec_lo, 0\n" \
  "s_brev_b32 exec_hi, 1\n" \
  "ds_write_b32 v172, v171\n" \
  "s_mov_b64 exec, -1\n" \
  "v_sub_u32_e32 v170, v171, v170\n" \
  "s_waitcnt lgkmcnt(0)\n" \
  "s_barrier\n" \
  "ds_read_b32 v171, v108\n" \
  "s_mov_b32 vcc_lo, 0xaaaaaaaa\n" \
  "s_mov_b32 vcc_hi, 0xaaaaaaaa\n" \
  "s_waitcnt lgkmcnt(0)\n" \
  "v_and_b32_e32 v171, v109, v171\n" \
  "s_nop 1\n" \
  "v_add_u32_dpp v171, v171, v171 row_ror:4 row_mask:0xf bank_mask:0xf\n" \
  "s_nop 1\n" \
  "v_add_u32_dpp v171, v171, v171 row_ror:2 row_mask:0xf bank_mask:0xf\n" \
  "s_nop 1\n" \
  "v_add_u32_dpp v171, v171, v171 row_ror:1 row_mask:0xf bank_mask:0xf\n" \
  "s_nop 0\n" \
  "v_add_u32_e32 v170, v170, v171\n" \
  "v_mad_u32_u24 v171, v168, v107, v170\n" \
  "ds_write2st64_b32 v102, v170, v171 offset0:144 offset1:152\n" \
  "v_bfe_u32 v170, v170, 2, 14\n" \
  "v_bfe_u32 v171, v171, 2, 14\n" \
  "v_lshl_add_u32 v168, v168, 14, v170\n" \
  "v_lshl_add_u32 v169, v169, 14, v171\n" \
  "s_waitcnt lgkmcnt(0)\n" \
  "s_barrier\n" \
  "s_cmp_eq_u32 %[wv1k], 7168\n" \
  "s_cbranch_scc1 W7C_%=\n" \
  "ds_read_b32 v112, v112 offset:36864\n" \
  "ds_read_b32 v113, v113 offset:36864\n" \
  "ds_read_b32 v114, v114 offset:36864\n" \
  "s_mov_b64 exec, s[60:61]\n" \
  "ds_read_b32 v115, v115 offset:36868\n" \
  "s_mov_b64 exec, s[62:63]\n" \
  "ds_read_b32 v116, v116 offset:36868\n" \
  "ds_read_b32 v117, v117 offset:36868\n" \
  "ds_read_b32 v118, v118 offset:36868\n" \
  "s_mov_b64 exec, -1\n" \
  "W7C_%=:\n" \
  "v_mov_b32_dpp v170, v168 quad_perm:[1,0,3,2] row_mask:0xf bank_mask:0xf\n" \
  "v_mov_b32_dpp v171, v169 quad_perm:[1,0,3,2] row_mask:0xf bank_mask:0xf\n" \
  "s_mov_b32 s68, 0x05040100\n" \
  "s_mov_b32 s69, 0x07060302\n" \
  "v_cndmask_b32_e32 v168, v168, v171, vcc\n" \
  "v_cndmask_b32_e32 v169, v170, v169, vcc\n" \
  "v_perm_b32 v170, v169, v168, s68\n" \
  "v_perm_b32 v171, v169, v168, s69\n" \
  "v_add_u32_e32 v170, 0x02000200, v170\n" \
  "s_cbranch_scc1 W7D_%=\n" \
  "s_waitcnt lgkmcnt(0)\n" \
  "v_add_u32_sdwa v168, v112, v120 dst_sel:DWORD dst_unused:UNUSED_PAD src0_sel:WORD_1 src1_sel:DWORD\n" \
  "ds_write_b32 v168, v104 offset:53504\n" \
  "v_lshl_or_b32 v128, v168, 3, v128\n" \
  "v_add_u32_e32 v104, 32, v104\n" \
  "v_add_u32_sdwa v112, v112, v120 dst_sel:DWORD dst_unused:UNUSED_PAD src0_sel:WORD_0 src1_sel:DWORD\n" \
  "ds_write_b32 v112, v128 offset:40960\n" \
  "v_add_u32_sdwa v168, v113, v121 dst_sel:DWORD dst_unused:UNUSED_PAD src0_sel:WORD_1 src1_sel:DWORD\n" \
  "ds_write_b32 v168, v104 offset:53504\n" \
  "v_lshl_or_b32 v129, v168, 3, v129\n" \
  "v_add_u32_e32 v104, 32, v104\n" \
  "v_add_u32_sdwa v113, v113, v121 dst_sel:DWORD dst_unused:UNUSED_PAD src0_sel:WORD_0 src1_sel:DWORD\n" \
  "ds_write_b32 v113, v129 offset:40960\n" \
  "v_add_u32_sdwa v168, v114, v122 dst_sel:DWORD dst_unused:UNUSED_PAD src0_sel:WORD_1 src1_sel:DWORD\n" \
  "ds_write_b32 v168, v104 offset:53504\n" \
  "v_lshl_or_b32 v130, v168, 3, v130\n" \
  "v_add_u32_sdwa v114, v114, v122 dst_sel:DWORD dst_unused:UNUSED_PAD src0_sel:WORD_0 src1_sel:DWORD\n" \
  "ds_write_b32 v114, v130 offset:40960\n" \
  "v_add_u32_sdwa v115, v115, v123 dst_sel:DWORD dst_unused:UNUSED_PAD src0_sel:WORD_0 src1_sel:DWORD\n" \
  "s_mov_b64 exec, s[60:61]\n" \
  "ds_write_b32 v115, v131 offset:40960\n" \
  "v_add_u32_sdwa v116, v116, v124 dst_sel:DWORD dst_unused:UNUSED_PAD src0_sel:WORD_0 src1_sel:DWORD\n" \
  "s_mov_b64 exec, s[62:63]\n" \
  "ds_write_b32 v116, v132 offset:40960\n" \
  "v_add_u32_sdwa v117, v117, v125 dst_sel:DWORD dst_unused:UNUSED_PAD src0_sel:WORD_0 src1_sel:DWORD\n" \
  "ds_write_b32 v117, v133 offset:40960\n" \
  "v_add_u32_sdwa v118, v118, v126 dst_sel:DWORD dst_unused:UNUSED_PAD src0_sel:WORD_0 src1_sel:DWORD\n" \
  "ds_write_b32 v118, v134 offset:40960\n" \
  "s_mov_b64 exec, -1\n" \
  "W7D_%=:\n" \
  "s_waitcnt vmcnt(2) lgkmcnt(0)\n" \
  "s_barrier\n" \
  "buffer_store_dwordx2 v[170:171], v110, %[rws], %[co] offen sc1\n" \
  "ds_read_b128 v[0:3], v105 offset:40960\n" \
  "ds_read_b128 v[4:7], v105 offset:49152\n" \
  "ds_read_b32 v8, v106\n" \
  "ds_read_b32 v9, v106 offset:1024\n" \
  "ds_read_b32 v10, v106 offset:2048\n" \
  "ds_read_b32 v11, v106 offset:3072\n" \
  "ds_read_b32 v12, v106 offset:4096\n" \
  "ds_read_b32 v13, v106 offset:5120\n" \
  "v_cmp_gt_u32_e32 vcc, 4352, v105\n" \
  "s_add_u32 s68, %[io], 8192\n" \
  "s_waitcnt lgkmcnt(6)\n" \
  "buffer_store_dwordx4 v[0:3], v105, %[rws], %[io] offen sc1\n" \
  "s_cbranch_vccz NI_%=\n" \
  "s_mov_b64 exec, vcc\n" \
  "buffer_store_dwordx4 v[4:7], v105, %[rws], s68 offen sc1\n" \
  "s_mov_b64 exec, -1\n" \
  "NI_%=:\n" \
  "s_waitcnt lgkmcnt(0)\n" \
  "v_add_u32_e32 v249, v8, v73\n" \
  "v_add_u32_e32 v250, v9, v73\n" \
  "v_add_u32_e32 v251, v10, v73\n" \
  "v_add_u32_e32 v252, v11, v73\n" \
  "v_add_u32_e32 v253, v12, v73\n" \
  "v_add_u32_e32 v254, v13, v73\n" \
  "ds_read_b128 v[190:193], v241\n" \
  "ds_read_b128 v[206:209], v242\n" \
  "v_xor_b32_e32 v168, 32, v241\n" \
  "v_xor_b32_e32 v169, 32, v242\n" \
  "ds_read_b128 v[194:197], v168\n" \
  "ds_read_b128 v[210:213], v169\n" \
  "v_xor_b32_e32 v168, 64, v241\n" \
  "v_xor_b32_e32 v169, 64, v242\n" \
  "ds_read_b128 v[198:201], v168\n" \
  "ds_read_b128 v[214:217], v169\n" \
  "v_xor_b32_e32 v168, 96, v241\n" \
  "v_xor_b32_e32 v169, 96, v242\n" \
  "ds_read_b128 v[202:205], v168\n" \
  "ds_read_b128 v[218:221], v169\n" \
  "ds_read_b64_tr_b16 v[0:1], v243 offset:16384\n" \
  "ds_read_b64_tr_b16 v[2:3], v243 offset:16896\n" \
  "ds_read_b64_tr_b16 v[4:5], v244 offset:16384\n" \
  "ds_read_b64_tr_b16 v[6:7], v244 offset:16896\n" \
  "s_waitcnt lgkmcnt(4)\n" \
  "ds_read_b64_tr_b16 v[8:9], v243 offset:18432\n" \
  "ds_read_b64_tr_b16 v[10:11], v243 offset:18944\n" \
  "ds_read_b64_tr_b16 v[12:13], v244 offset:18432\n" \
  "ds_read_b64_tr_b16 v[14:15], v244 offset:18944\n" \
  "ds_read_b64_tr_b16 v[16:17], v243 offset:20480\n" \
  "ds_read_b64_tr_b16 v[18:19], v243 offset:20992\n" \
  "ds_read_b64_tr_b16 v[20:21], v244 offset:20480\n" \
  "ds_read_b64_tr_b16 v[22:23], v244 offset:20992\n" \
  "s_waitcnt lgkmcnt(8)\n" \
  "v_mfma_f32_32x32x16_bf16 v[48:63], v[0:3], v[190:193], 0\n" \
  "v_mfma_f32_32x32x16_bf16 v[48:63], v[4:7], v[206:209], v[48:63]\n" \
  "ds_read_b64_tr_b16 v[0:1], v243 offset:22528\n" \
  "ds_read_b64_tr_b16 v[2:3], v243 offset:23040\n" \
  "ds_read_b64_tr_b16 v[4:5], v244 offset:22528\n" \
  "ds_read_b64_tr_b16 v[6:7], v244 offset:23040\n" \
  "s_waitcnt lgkmcnt(8)\n" \
  "v_mfma_f32_32x32x16_bf16 v[48:63], v[8:11], v[194:197], v[48:63]\n" \
  "v_mfma_f32_32x32x16_bf16 v[48:63], v[12:15], v[210:213], v[48:63]\n" \
  "s_waitcnt lgkmcnt(4)\n" \
  "v_mfma_f32_32x32x16_bf16 v[48:63], v[16:19], v[198:201], v[48:63]\n" \
  "v_mfma_f32_32x32x16_bf16 v[48:63], v[20:23], v[214:217], v[48:63]\n" \
  "s_waitcnt lgkmcnt(0)\n" \
  "v_mfma_f32_32x32x16_bf16 v[48:63], v[0:3], v[202:205], v[48:63]\n" \
  "v_mfma_f32_32x32x16_bf16 v[48:63], v[4:7], v[218:221], v[48:63]\n" \
  "s_nop 11\n" \
  "v_mul_f32_e32 v48, %[nl2e], v48\n" \
  "v_mul_f32_e32 v49, %[nl2e], v49\n" \
  "v_mul_f32_e32 v50, %[nl2e], v50\n" \
  "v_mul_f32_e32 v51, %[nl2e], v51\n" \
  "v_mul_f32_e32 v52, %[nl2e], v52\n" \
  "v_mul_f32_e32 v53, %[nl2e], v53\n" \
  "v_mul_f32_e32 v54, %[nl2e], v54\n" \
  "v_mul_f32_e32 v55, %[nl2e], v55\n" \
  "v_mul_f32_e32 v56, %[nl2e], v56\n" \
  "v_mul_f32_e32 v57, %[nl2e], v57\n" \
  "v_mul_f32_e32 v58, %[nl2e], v58\n" \
  "v_mul_f32_e32 v59, %[nl2e], v59\n" \
  "v_mul_f32_e32 v60, %[nl2e], v60\n" \
  "v_mul_f32_e32 v61, %[nl2e], v61\n" \
  "v_mul_f32_e32 v62, %[nl2e], v62\n" \
  "v_mul_f32_e32 v63, %[nl2e], v63\n" \
  "v_cvt_pk_bf16_f32 v48, v48, v49\n" \
  "v_cvt_pk_bf16_f32 v49, v50, v51\n" \
  "v_cvt_pk_bf16_f32 v50, v52, v53\n" \
  "v_cvt_pk_bf16_f32 v51, v54, v55\n" \
  "v_cvt_pk_bf16_f32 v52, v56, v57\n" \
  "v_cvt_pk_bf16_f32 v53, v58, v59\n" \
  "v_cvt_pk_bf16_f32 v54, v60, v61\n" \
  "v_cvt_pk_bf16_f32 v55, v62, v63\n" \
  "v_xor_b32_e32 v168, 64, v245\n" \
  "ds_write_b128 v245, v[48:51]\n" \
  "ds_write_b128 v168, v[52:55]\n" \
  "ds_read_b128 v[112:115], v246\n" \
  "ds_read_b128 v[116:119], v246 offset:1024\n" \
  "s_waitcnt vmcnt(0)\n" \
  "s_barrier\n" \
  "s_cmp_lg_u32 %[wv1k], 0\n" \
  "s_cbranch_scc1 FA_%=\n" \
  "v_mov_b32_e32 v168, s78\n" \
  "v_mov_b32_e32 v169, s79\n" \
  "v_lshrrev_b32_e32 v170, 1, v88\n" \
  "s_mov_b64 exec, 0xffff\n" \
  "buffer_store_dwordx2 v[168:169], v170, %[rws], %[fo] offen sc1\n" \
  "s_mov_b64 exec, -1\n" \
  "FA_%=:\n" \
  "s_waitcnt lgkmcnt(0)\n" \
  "buffer_store_dwordx4 v[112:115], v247, %[rws], 0 offen sc1\n" \
  "buffer_store_dwordx4 v[116:119], v248, %[rws], 0 offen sc1\n" \
  "ds_read_b128 v[136:139], v249\n" \
  "ds_read_b128 v[140:143], v250\n" \
  "ds_read_b128 v[144:147], v251\n" \
  "ds_read_b128 v[148:151], v252\n" \
  "ds_read_b128 v[152:155], v253\n" \
  "s_cmp_gt_u32 %[wv1k], 1024\n" \
  "s_cbranch_scc1 G5_%=\n" \
  "ds_read_b128 v[156:159], v254\n" \
  "G5_%=:\n" \
  "s_waitcnt lgkmcnt(0)\n" \
  "s_cmp_gt_u32 %[wv1k], 1024\n" \
  "s_cbranch_scc1 G6_%=\n" \
  "s_add_u32 s68, %[po], 40960\n" \
  "buffer_store_dwordx4 v[156:159], v105, %[rws], s68 offen sc1\n" \
  "G6_%=:\n" \
  "s_add_u32 s68, %[po], 0\n" \
  "buffer_store_dwordx4 v[136:139], v105, %[rws], s68 offen sc1\n" \
  "s_add_u32 s68, %[po], 8192\n" \
  "buffer_store_dwordx4 v[140:143], v105, %[rws], s68 offen sc1\n" \
  "s_add_u32 s68, %[po], 16384\n" \
  "buffer_store_dwordx4 v[144:147], v105, %[rws], s68 offen sc1\n" \
  "s_add_u32 s68, %[po], 24576\n" \
  "buffer_store_dwordx4 v[148:151], v105, %[rws], s68 offen sc1\n" \
  "s_add_u32 s68, %[po], 32768\n" \
  "buffer_store_dwordx4 v[152:155], v105, %[rws], s68 offen sc1\n" \
  "ds_read_b64_tr_b16 v[0:1], v243\n" \
  "ds_read_b64_tr_b16 v[2:3], v243 offset:512\n" \
  "ds_read_b64_tr_b16 v[4:5], v244\n" \
  "ds_read_b64_tr_b16 v[6:7], v244 offset:512\n" \
  "s_waitcnt lgkmcnt(4)\n" \
  "ds_read_b64_tr_b16 v[8:9], v243 offset:2048\n" \
  "ds_read_b64_tr_b16 v[10:11], v243 offset:2560\n" \
  "ds_read_b64_tr_b16 v[12:13], v244 offset:2048\n" \
  "ds_read_b64_tr_b16 v[14:15], v244 offset:2560\n" \
  "ds_read_b64_tr_b16 v[16:17], v243 offset:4096\n" \
  "ds_read_b64_tr_b16 v[18:19], v243 offset:4608\n" \
  "ds_read_b64_tr_b16 v[20:21], v244 offset:4096\n" \
  "ds_read_b64_tr_b16 v[22:23], v244 offset:4608\n" \
  "s_waitcnt lgkmcnt(8)\n" \
  "v_mfma_f32_32x32x16_bf16 v[222:237], v[0:3], v[190:193], 0\n" \
  "v_mfma_f32_32x32x16_bf16 v[222:237], v[4:7], v[206:209], v[222:237]\n" \
  "ds_read_b64_tr_b16 v[0:1], v243 offset:6144\n" \
  "ds_read_b64_tr_b16 v[2:3], v243 offset:6656\n" \
  "ds_read_b64_tr_b16 v[4:5], v244 offset:6144\n" \
  "ds_read_b64_tr_b16 v[6:7], v244 offset:6656\n" \
  "s_waitcnt lgkmcnt(8)\n" \
  "v_mfma_f32_32x32x16_bf16 v[222:237], v[8:11], v[194:197], v[222:237]\n" \
  "v_mfma_f32_32x32x16_bf16 v[222:237], v[12:15], v[210:213], v[222:237]\n" \
  "ds_read_b64_tr_b16 v[8:9], v243 offset:24576\n" \
  "ds_read_b64_tr_b16 v[10:11], v243 offset:25088\n" \
  "ds_read_b64_tr_b16 v[12:13], v244 offset:24576\n" \
  "ds_read_b64_tr_b16 v[14:15], v244 offset:25088\n" \
  "s_waitcnt lgkmcnt(8)\n" \
  "v_mfma_f32_32x32x16_bf16 v[222:237], v[16:19], v[198:201], v[222:237]\n" \
  "v_mfma_f32_32x32x16_bf16 v[222:237], v[20:23], v[214:217], v[222:237]\n" \
  "ds_read_b64_tr_b16 v[16:17], v243 offset:26624\n" \
  "ds_read_b64_tr_b16 v[18:19], v243 offset:27136\n" \
  "ds_read_b64_tr_b16 v[20:21], v244 offset:26624\n" \
  "ds_read_b64_tr_b16 v[22:23], v244 offset:27136\n" \
  "s_waitcnt lgkmcnt(8)\n" \
  "v_mfma_f32_32x32x16_bf16 v[222:237], v[0:3], v[202:205], v[222:237]\n" \
  "v_mfma_f32_32x32x16_bf16 v[222:237], v[4:7], v[218:221], v[222:237]\n" \
  "ds_read_b64_tr_b16 v[0:1], v243 offset:28672\n" \
  "ds_read_b64_tr_b16 v[2:3], v243 offset:29184\n" \
  "ds_read_b64_tr_b16 v[4:5], v244 offset:28672\n" \
  "ds_read_b64_tr_b16 v[6:7], v244 offset:29184\n" \
  "s_waitcnt lgkmcnt(8)\n" \
  "v_mfma_f32_32x32x16_bf16 v[48:63], v[190:193], v[8:11], 0\n" \
  "v_mfma_f32_32x32x16_bf16 v[160:175], v[190:193], v[12:15], 0\n" \
  "ds_read_b64_tr_b16 v[8:9], v243 offset:30720\n" \
  "ds_read_b64_tr_b16 v[10:11], v243 offset:31232\n" \
  "ds_read_b64_tr_b16 v[12:13], v244 offset:30720\n" \
  "ds_read_b64_tr_b16 v[14:15], v244 offset:31232\n" \
  "s_waitcnt lgkmcnt(8)\n" \
  "v_mfma_f32_32x32x16_bf16 v[48:63], v[194:197], v[16:19], v[48:63]\n" \
  "v_mfma_f32_32x32x16_bf16 v[160:175], v[194:197], v[20:23], v[160:175]\n" \
  "ds_read_b64_tr_b16 v[16:17], v243 offset:32768\n" \
  "ds_read_b64_tr_b16 v[18:19], v243 offset:33280\n" \
  "ds_read_b64_tr_b16 v[20:21], v244 offset:32768\n" \
  "ds_read_b64_tr_b16 v[22:23], v244 offset:33280\n" \
  "s_waitcnt lgkmcnt(8)\n" \
  "v_mfma_f32_32x32x16_bf16 v[48:63], v[198:201], v[0:3], v[48:63]\n" \
  "v_mfma_f32_32x32x16_bf16 v[160:175], v[198:201], v[4:7], v[160:175]\n" \
  "s_waitcnt lgkmcnt(4)\n" \
  "v_mfma_f32_32x32x16_bf16 v[48:63], v[202:205], v[8:11], v[48:63]\n" \
  "v_mfma_f32_32x32x16_bf16 v[160:175], v[202:205], v[12:15], v[160:175]\n" \
  "s_waitcnt lgkmcnt(0)\n" \
  "v_lshlrev_b32_e32 v0, 16, v16\n" \
  "v_and_b32_e32 v1, 0xffff0000, v16\n" \
  "v_mul_f32_e32 v0, %[nl2e], v0\n" \
  "v_mul_f32_e32 v1, %[nl2e], v1\n" \
  "v_cvt_pk_bf16_f32 v32, v0, v1\n" \
  "v_lshlrev_b32_e32 v0, 16, v17\n" \
  "v_and_b32_e32 v1, 0xffff0000, v17\n" \
  "v_mul_f32_e32 v0, %[nl2e], v0\n" \
  "v_mul_f32_e32 v1, %[nl2e], v1\n" \
  "v_cvt_pk_bf16_f32 v33, v0, v1\n" \
  "v_lshlrev_b32_e32 v0, 16, v18\n" \
  "v_and_b32_e32 v1, 0xffff0000, v18\n" \
  "v_mul_f32_e32 v0, %[nl2e], v0\n" \
  "v_mul_f32_e32 v1, %[nl2e], v1\n" \
  "v_cvt_pk_bf16_f32 v34, v0, v1\n" \
  "v_lshlrev_b32_e32 v0, 16, v19\n" \
  "v_and_b32_e32 v1, 0xffff0000, v19\n" \
  "v_mul_f32_e32 v0, %[nl2e], v0\n" \
  "v_mul_f32_e32 v1, %[nl2e], v1\n" \
  "v_cvt_pk_bf16_f32 v35, v0, v1\n" \
  "v_lshlrev_b32_e32 v0, 16, v20\n" \
  "v_and_b32_e32 v1, 0xffff0000, v20\n" \
  "v_mul_f32_e32 v0, %[nl2e], v0\n" \
  "v_mul_f32_e32 v1, %[nl2e], v1\n" \
  "v_cvt_pk_bf16_f32 v36, v0, v1\n" \
  "v_lshlrev_b32_e32 v0, 16, v21\n" \
  "v_and_b32_e32 v1, 0xffff0000, v21\n" \
  "v_mul_f32_e32 v0, %[nl2e], v0\n" \
  "v_mul_f32_e32 v1, %[nl2e], v1\n" \
  "v_cvt_pk_bf16_f32 v37, v0, v1\n" \
  "v_lshlrev_b32_e32 v0, 16, v22\n" \
  "v_and_b32_e32 v1, 0xffff0000, v22\n" \
  "v_mul_f32_e32 v0, %[nl2e], v0\n" \
  "v_mul_f32_e32 v1, %[nl2e], v1\n" \
  "v_cvt_pk_bf16_f32 v38, v0, v1\n" \
  "v_lshlrev_b32_e32 v0, 16, v23\n" \
  "v_and_b32_e32 v1, 0xffff0000, v23\n" \
  "v_mul_f32_e32 v0, %[nl2e], v0\n" \
  "v_mul_f32_e32 v1, %[nl2e], v1\n" \
  "v_cvt_pk_bf16_f32 v39, v0, v1\n" \
  "v_mul_f32_e32 v222, %[nl2e], v222\n" \
  "v_mul_f32_e32 v223, %[nl2e], v223\n" \
  "v_mul_f32_e32 v224, %[nl2e], v224\n" \
  "v_mul_f32_e32 v225, %[nl2e], v225\n" \
  "v_mul_f32_e32 v226, %[nl2e], v226\n" \
  "v_mul_f32_e32 v227, %[nl2e], v227\n" \
  "v_mul_f32_e32 v228, %[nl2e], v228\n" \
  "v_mul_f32_e32 v229, %[nl2e], v229\n" \
  "v_mul_f32_e32 v230, %[nl2e], v230\n" \
  "v_mul_f32_e32 v231, %[nl2e], v231\n" \
  "v_mul_f32_e32 v232, %[nl2e], v232\n" \
  "v_mul_f32_e32 v233, %[nl2e], v233\n" \
  "v_mul_f32_e32 v234, %[nl2e], v234\n" \
  "v_mul_f32_e32 v235, %[nl2e], v235\n" \
  "v_mul_f32_e32 v236, %[nl2e], v236\n" \
  "v_mul_f32_e32 v237, %[nl2e], v237\n" \
  "v_mul_f32_e32 v48, %[nl2e], v48\n" \
  "v_mul_f32_e32 v160, %[nl2e], v160\n" \
  "v_mul_f32_e32 v49, %[nl2e], v49\n" \
  "v_mul_f32_e32 v161, %[nl2e], v161\n" \
  "v_mul_f32_e32 v50, %[nl2e], v50\n" \
  "v_mul_f32_e32 v162, %[nl2e], v162\n" \
  "v_mul_f32_e32 v51, %[nl2e], v51\n" \
  "v_mul_f32_e32 v163, %[nl2e], v163\n" \
  "v_mul_f32_e32 v52, %[nl2e], v52\n" \
  "v_mul_f32_e32 v164, %[nl2e], v164\n" \
  "v_mul_f32_e32 v53, %[nl2e], v53\n" \
  "v_mul_f32_e32 v165, %[nl2e], v165\n" \
  "v_mul_f32_e32 v54, %[nl2e], v54\n" \
  "v_mul_f32_e32 v166, %[nl2e], v166\n" \
  "v_mul_f32_e32 v55, %[nl2e], v55\n" \
  "v_mul_f32_e32 v167, %[nl2e], v167\n" \
  "v_cvt_pk_bf16_f32 v40, v48, v49\n" \
  "v_cvt_pk_bf16_f32 v44, v160, v161\n" \
  "v_cvt_pk_bf16_f32 v41, v50, v51\n" \
  "v_cvt_pk_bf16_f32 v45, v162, v163\n" \
  "v_cvt_pk_bf16_f32 v42, v52, v53\n" \
  "v_cvt_pk_bf16_f32 v46, v164, v165\n" \
  "v_cvt_pk_bf16_f32 v43, v54, v55\n" \
  "v_cvt_pk_bf16_f32 v47, v166, v167\n" \
  "s_mov_b32 s68, 0x40000\n" \
  "s_mov_b32 exec_lo, 0x55555555\n" \
  "s_mov_b32 exec_hi, 0x55555555\n" \
  "PA_%=:\n" \
  "buffer_load_dwordx2 v[168:169], v100, %[rws], 0 offen sc1\n" \
  "s_waitcnt vmcnt(0)\n" \
  "v_cmpx_ne_u64_e32 vcc, s[78:79], v[168:169]\n" \
  "s_cbranch_execz PAD_%=\n" \
  "s_sleep 2\n" \
  "s_sub_u32 s68, s68, 1\n" \
  "s_cbranch_scc0 PA_%=\n" \
  "s_endpgm\n" \
  "PAD_%=:\n" \
  "s_mov_b64 exec, -1\n" \
  "buffer_load_dwordx2 v[80:81], v101, %[rws], 0 offen sc1\n" \
  "s_barrier\n" \
  "s_cmp_lg_u32 %[wv1k], 0\n" \
  "s_cbranch_scc1 FB_%=\n" \
  "v_mov_b32_e32 v168, s78\n" \
  "v_mov_b32_e32 v169, s79\n" \
  "v_lshrrev_b32_e32 v170, 1, v88\n" \
  "s_mov_b64 exec, 0xffff\n" \
  "buffer_store_dwordx2 v[168:169], v170, %[rws], %[fo] offen offset:128 sc1\n" \
  "s_mov_b64 exec, -1\n" \
  "FB_%=:\n" \
  "s_waitcnt vmcnt(1)\n" \
  "s_cmp_eq_u32 %[wv1k], 0\n" \
  "s_cbranch_scc1 CW_%=\n" \
  "s_waitcnt vmcnt(0)\n" \
  "CW_%=:\n" \
  "v_mov_b32_e32 v168, v81\n" \
  "v_and_b32_e32 v169, 0xfc00fc00, v81\n" \
  "v_cmp_ne_u32_e32 vcc, 0, v169\n" \
  "v_add_u32_dpp v168, v168, v168 row_shr:1 row_mask:0xf bank_mask:0xf bound_ctrl:1\n" \
  "v_mov_b32_e32 v176, %[pada]\n" \
  "v_mov_b32_e32 v177, %[pada]\n" \
  "v_add_u32_dpp v168, v168, v168 row_shr:2 row_mask:0xf bank_mask:0xf bound_ctrl:1\n" \
  "v_mov_b32_e32 v178, %[pada]\n" \
  "v_mov_b32_e32 v179, %[pada]\n" \
  "v_add_u32_dpp v168, v168, v168 row_shr:4 row_mask:0xf bank_mask:0xf bound_ctrl:1\n" \
  "v_mov_b32_e32 v186, -1\n" \
  "v_mov_b32_e32 v187, -1\n" \
  "v_add_u32_dpp v168, v168, v168 row_shr:8 row_mask:0xf bank_mask:0xf bound_ctrl:1\n" \
  "v_mov_b32_e32 v188, -1\n" \
  "v_mov_b32_e32 v189, -1\n" \
  "v_add_u32_dpp v168, v168, v168 row_bcast:15 row_mask:0xa bank_mask:0xf\n" \
  "v_mov_b32_e32 v180, 0x80\n" \
  "s_nop 0\n" \
  "v_add_u32_dpp v168, v168, v168 row_bcast:31 row_mask:0xc bank_mask:0xf\n" \
  "s_nop 1\n" \
  "v_readlane_b32 s68, v168, 63\n" \
  "v_sub_u32_e32 v168, v168, v81\n" \
  "s_and_b32 %[tp], s68, 0xffff\n" \
  "s_lshr_b32 %[tot], s68, 16\n" \
  "s_lshl_b32 s68, s68, 16\n" \
  "s_add_u32 %[tot], %[tot], %[tp]\n" \
  "v_add_u32_e32 v168, s68, v168\n" \
  "v_sub_u32_e32 v169, v80, v168\n" \
  "s_movk_i32 s69, 0x2000\n" \
  "v_mad_u32_u16 v170, v168, 4, %[mkb]\n" \
  "v_mad_u32_u16 v171, v168, 4, %[mkb] op_sel:[1,0,0,0]\n" \
  "v_mad_u32_u16 v172, v169, s69, v82\n" \
  "v_mad_u32_u16 v173, v169, s69, v83 op_sel:[1,0,0,0]\n" \
  "s_cbranch_vccnz IRR_%=\n" \
  "s_sub_u32 s68, %[tot], 256\n" \
  "s_cmp_gt_u32 s68, 255\n" \
  "s_cbranch_scc1 IRR_%=\n" \
  "s_cmp_gt_u32 %[tp], 256\n" \
  "s_cbranch_scc1 IRR_%=\n" \
  "ds_max_u32 v170, v172\n" \
  "ds_max_u32 v171, v173\n" \
  "REG_%=:\n" \
  "ds_read_b128 v[112:115], v85\n" \
  "ds_read_b128 v[116:119], v85 offset:16\n" \
  "s_sub_i32 s68, %[tot], 256\n" \
  "v_cmp_gt_i32_e64 s[70:71], s68, v84\n" \
  "s_sub_i32 s68, %[tot], 320\n" \
  "v_cmp_gt_i32_e64 s[72:73], s68, v84\n" \
  "s_sub_i32 s68, %[tot], 384\n" \
  "v_cmp_gt_i32_e64 s[74:75], s68, v84\n" \
  "s_sub_i32 s68, %[tot], 448\n" \
  "v_cmp_gt_i32_e64 s[76:77], s68, v84\n" \
  "s_waitcnt lgkmcnt(0)\n" \
  "v_max3_u32 v114, v112, v113, v114\n" \
  "v_max3_u32 v116, v114, v115, v116\n" \
  "v_max3_u32 v118, v116, v117, v118\n" \
  "v_max_u32_e32 v168, v118, v119\n" \
  "v_max_u32_e32 v113, v112, v113\n" \
  "v_max_u32_e32 v115, v114, v115\n" \
  "v_max_u32_dpp v168, v168, v168 row_shr:1 row_mask:0xf bank_mask:0xf bound_ctrl:1\n" \
  "v_max_u32_e32 v117, v116, v117\n" \
  "s_nop 0\n" \
  "v_max_u32_dpp v168, v168, v168 row_shr:2 row_mask:0xf bank_mask:0xf bound_ctrl:1\n" \
  "s_nop 1\n" \
  "v_max_u32_dpp v168, v168, v168 row_shr:4 row_mask:0xf bank_mask:0xf bound_ctrl:1\n" \
  "s_nop 1\n" \
  "v_max_u32_dpp v168, v168, v168 row_shr:8 row_mask:0xf bank_mask:0xf bound_ctrl:1\n" \
  "s_nop 1\n" \
  "v_max_u32_dpp v168, v168, v168 row_bcast:15 row_mask:0xa bank_mask:0xf\n" \
  "s_nop 1\n" \
  "v_max_u32_dpp v168, v168, v168 row_bcast:31 row_mask:0xc bank_mask:0xf\n" \
  "s_nop 1\n" \
  "v_max_u32_dpp v112, v168, v112 wave_shr:1 row_mask:0xf bank_mask:0xf bound_ctrl:1\n" \
  "v_max_u32_dpp v113, v168, v113 wave_shr:1 row_mask:0xf bank_mask:0xf bound_ctrl:1\n" \
  "v_max_u32_dpp v114, v168, v114 wave_shr:1 row_mask:0xf bank_mask:0xf bound_ctrl:1\n" \
  "v_max_u32_dpp v115, v168, v115 wave_shr:1 row_mask:0xf bank_mask:0xf bound_ctrl:1\n" \
  "v_max_u32_dpp v116, v168, v116 wave_shr:1 row_mask:0xf bank_mask:0xf bound_ctrl:1\n" \
  "v_max_u32_dpp v117, v168, v117 wave_shr:1 row_mask:0xf bank_mask:0xf bound_ctrl:1\n" \
  "v_max_u32_dpp v118, v168, v118 wave_shr:1 row_mask:0xf bank_mask:0xf bound_ctrl:1\n" \
  "v_mov_b32_e32 v119, v168\n" \
  "ds_write_b128 v85, v[112:115]\n" \
  "ds_write_b128 v85, v[116:119] offset:16\n" \
  "ds_read2st64_b32 v[112:113], v86 offset0:0 offset1:1\n" \
  "ds_read2st64_b32 v[114:115], v86 offset0:2 offset1:3\n" \
  "ds_read2st64_b32 v[116:117], v86 offset0:4 offset1:5\n" \
  "ds_read2st64_b32 v[118:119], v86 offset0:6 offset1:7\n" \
  "s_waitcnt lgkmcnt(3)\n" \
  "v_bfe_u32 v128, v112, 13, 17\n" \
  "v_add_lshl_u32 v128, v128, v84, 2\n" \
  "buffer_load_dword v120, v128, %[rws], %[ib] offen sc1\n" \
  "v_bfe_u32 v129, v113, 13, 17\n" \
  "v_add_lshl_u32 v129, v129, v84, 2\n" \
  "buffer_load_dword v121, v129, %[rws], %[ib] offen offset:256 sc1\n" \
  "s_waitcnt lgkmcnt(2)\n" \
  "v_bfe_u32 v130, v114, 13, 17\n" \
  "v_add_lshl_u32 v130, v130, v84, 2\n" \
  "buffer_load_dword v122, v130, %[rws], %[ib] offen offset:512 sc1\n" \
  "v_bfe_u32 v131, v115, 13, 17\n" \
  "v_add_lshl_u32 v131, v131, v84, 2\n" \
  "buffer_load_dword v123, v131, %[rws], %[ib] offen offset:768 sc1\n" \
  "s_waitcnt lgkmcnt(1)\n" \
  "v_bfe_u32 v132, v116, 13, 17\n" \
  "v_add_lshl_u32 v132, v132, v84, 2\n" \
  "buffer_load_dword v124, v132, %[rws], %[ib] offen offset:1024 sc1\n" \
  "v_bfe_u32 v133, v117, 13, 17\n" \
  "v_add_lshl_u32 v133, v133, v84, 2\n" \
  "buffer_load_dword v125, v133, %[rws], %[ib] offen offset:1280 sc1\n" \
  "s_cmp_lt_u32 %[tot], 385\n" \
  "s_cbranch_scc1 LD_%=\n" \
  "s_waitcnt lgkmcnt(0)\n" \
  "v_bfe_u32 v134, v118, 13, 17\n" \
  "v_add_lshl_u32 v134, v134, v84, 2\n" \
  "buffer_load_dword v126, v134, %[rws], %[ib] offen offset:1536 sc1\n" \
  "s_cmp_lt_u32 %[tot], 449\n" \
  "s_cbranch_scc1 LD_%=\n" \
  "v_bfe_u32 v135, v119, 13, 17\n" \
  "v_add_lshl_u32 v135, v135, v84, 2\n" \
  "buffer_load_dword v127, v135, %[rws], %[ib] offen offset:1792 sc1\n" \
  "LD_%=:\n" \
  "s_mov_b32 exec_lo, 0x55555555\n" \
  "s_mov_b32 exec_hi, 0x55555555\n" \
  "buffer_load_dwordx2 v[174:175], v100, %[rws], 0 offen offset:128 sc1\n" \
  "s_mov_b64 exec, -1\n" \
  "s_mov_b32 s69, 100000\n" \
  "v_mul_u32_u24_sdwa v136, v112, s69 dst_sel:DWORD dst_unused:UNUSED_PAD src0_sel:BYTE_0 src1_sel:DWORD\n" \
  "v_cmp_gt_i32_e64 s[60:61], 0, v112\n" \
  "v_lshlrev_b32_e32 v144, 19, v112\n" \
  "v_and_b32_e32 v152, 0xe00, v112\n" \
  "ds_write_b128 v94, v[176:179]\n" \
  "v_mul_u32_u24_sdwa v137, v113, s69 dst_sel:DWORD dst_unused:UNUSED_PAD src0_sel:BYTE_0 src1_sel:DWORD\n" \
  "v_cmp_gt_i32_e64 s[62:63], 0, v113\n" \
  "v_lshlrev_b32_e32 v145, 19, v113\n" \
  "v_and_b32_e32 v153, 0xe00, v113\n" \
  "ds_write_b128 v94, v[176:179] offset:1024\n" \
  "v_mul_u32_u24_sdwa v138, v114, s69 dst_sel:DWORD dst_unused:UNUSED_PAD src0_sel:BYTE_0 src1_sel:DWORD\n" \
  "v_cmp_gt_i32_e64 s[64:65], 0, v114\n" \
  "v_lshlrev_b32_e32 v146, 19, v114\n" \
  "v_and_b32_e32 v154, 0xe00, v114\n" \
  "ds_write_b128 v94, v[176:179] offset:1536\n" \
  "v_mul_u32_u24_sdwa v139, v115, s69 dst_sel:DWORD dst_unused:UNUSED_PAD src0_sel:BYTE_0 src1_sel:DWORD\n" \
  "v_cmp_gt_i32_e64 s[66:67], 0, v115\n" \
  "v_lshlrev_b32_e32 v147, 19, v115\n" \
  "v_and_b32_e32 v155, 0xe00, v115\n" \
  "ds_write_b128 v95, v[186:189]\n" \
  "v_mul_u32_u24_sdwa v140, v116, s69 dst_sel:DWORD dst_unused:UNUSED_PAD src0_sel:BYTE_0 src1_sel:DWORD\n" \
  "v_lshlrev_b32_e32 v148, 19, v116\n" \
  "v_and_b32_e32 v156, 0xe00, v116\n" \
  "ds_write_b128 v95, v[186:189] offset:256\n" \
  "v_mul_u32_u24_sdwa v141, v117, s69 dst_sel:DWORD dst_unused:UNUSED_PAD src0_sel:BYTE_0 src1_sel:DWORD\n" \
  "v_lshlrev_b32_e32 v149, 19, v117\n" \
  "v_and_b32_e32 v157, 0xe00, v117\n" \
  "s_cmp_lt_u32 %[tot], 385\n" \
  "s_cbranch_scc1 SH_%=\n" \
  "v_mul_u32_u24_sdwa v142, v118, s69 dst_sel:DWORD dst_unused:UNUSED_PAD src0_sel:BYTE_0 src1_sel:DWORD\n" \
  "v_lshlrev_b32_e32 v150, 19, v118\n" \
  "v_and_b32_e32 v158, 0xe00, v118\n" \
  "s_cmp_lt_u32 %[tot], 449\n" \
  "s_cbranch_scc1 SH_%=\n" \
  "v_mul_u32_u24_sdwa v143, v119, s69 dst_sel:DWORD dst_unused:UNUSED_PAD src0_sel:BYTE_0 src1_sel:DWORD\n" \
  "v_lshlrev_b32_e32 v151, 19, v119\n" \
  "v_and_b32_e32 v159, 0xe00, v119\n" \
  "SH_%=:\n" \
  "s_movk_i32 s69, 0x1fc\n" \
  "s_waitcnt vmcnt(1)\n" \
  "v_alignbit_b32 v168, v120, v120, 28\n" \
  "v_and_or_b32 v152, v168, s69, v152\n" \
  "ds_add_rtn_u32 v128, v152, v180\n" \
  "v_alignbit_b32 v168, v121, v121, 28\n" \
  "v_and_or_b32 v153, v168, s69, v153\n" \
  "ds_add_rtn_u32 v129, v153, v180\n" \
  "v_alignbit_b32 v168, v122, v122, 28\n" \
  "v_and_or_b32 v154, v168, s69, v154\n" \
  "ds_add_rtn_u32 v130, v154, v180\n" \
  "v_alignbit_b32 v168, v123, v123, 28\n" \
  "v_and_or_b32 v155, v168, s69, v155\n" \
  "ds_add_rtn_u32 v131, v155, v180\n" \
  "v_alignbit_b32 v168, v124, v124, 28\n" \
  "v_and_or_b32 v156, v168, s69, v156\n" \
  "s_mov_b64 exec, s[70:71]\n" \
  "ds_add_rtn_u32 v132, v156, v180\n" \
  "v_alignbit_b32 v168, v125, v125, 28\n" \
  "v_and_or_b32 v157, v168, s69, v157\n" \
  "s_mov_b64 exec, s[72:73]\n" \
  "ds_add_rtn_u32 v133, v157, v180\n" \
  "s_cmp_lt_u32 %[tot], 385\n" \
  "s_cbranch_scc1 CN_%=\n" \
  "v_alignbit_b32 v168, v126, v126, 28\n" \
  "v_and_or_b32 v158, v168, s69, v158\n" \
  "s_mov_b64 exec, s[74:75]\n" \
  "ds_add_rtn_u32 v134, v158, v180\n" \
  "s_cmp_lt_u32 %[tot], 449\n" \
  "s_cbranch_scc1 CN_%=\n" \
  "v_alignbit_b32 v168, v127, v127, 28\n" \
  "v_and_or_b32 v159, v168, s69, v159\n" \
  "s_mov_b64 exec, s[76:77]\n" \
  "ds_add_rtn_u32 v135, v159, v180\n" \
  "CN_%=:\n" \
  "s_mov_b64 exec, -1\n" \
  "v_add_u32_sdwa v136, v120, v136 dst_sel:DWORD dst_unused:UNUSED_PAD src0_sel:WORD_0 src1_sel:DWORD\n" \
  "v_cndmask_b32_e64 v168, v136, 2.0, s[60:61]\n" \
  "ds_bpermute_b32 v160, v87, v168\n" \
  "ds_bpermute_b32 v161, v87, v168 offset:128\n" \
  "v_and_or_b32 v169, v120, 15, v96\n" \
  "v_cndmask_b32_e64 v136, v169, v136, s[60:61]\n" \
  "v_add_u32_sdwa v137, v121, v137 dst_sel:DWORD dst_unused:UNUSED_PAD src0_sel:WORD_0 src1_sel:DWORD\n" \
  "v_cndmask_b32_e64 v168, v137, 2.0, s[62:63]\n" \
  "ds_bpermute_b32 v162, v87, v168\n" \
  "ds_bpermute_b32 v163, v87, v168 offset:128\n" \
  "v_and_or_b32 v169, v121, 15, v97\n" \
  "v_cndmask_b32_e64 v137, v169, v137, s[62:63]\n" \
  "v_add_u32_sdwa v138, v122, v138 dst_sel:DWORD dst_unused:UNUSED_PAD src0_sel:WORD_0 src1_sel:DWORD\n" \
  "v_cndmask_b32_e64 v168, v138, 2.0, s[64:65]\n" \
  "ds_bpermute_b32 v164, v87, v168\n" \
  "ds_bpermute_b32 v165, v87, v168 offset:128\n" \
  "v_and_or_b32 v169, v122, 15, v98\n" \
  "v_cndmask_b32_e64 v138, v169, v138, s[64:65]\n" \
  "v_add_u32_sdwa v139, v123, v139 dst_sel:DWORD dst_unused:UNUSED_PAD src0_sel:WORD_0 src1_sel:DWORD\n" \
  "v_cndmask_b32_e64 v168, v139, 2.0, s[66:67]\n" \
  "ds_bpermute_b32 v166, v87, v168\n" \
  "ds_bpermute_b32 v167, v87, v168 offset:128\n" \
  "v_and_or_b32 v169, v123, 15, v99\n" \
  "v_cndmask_b32_e64 v139, v169, v139, s[66:67]\n" \
  "s_mov_b32 s68, 0x40000\n" \
  "s_mov_b32 exec_lo, 0x55555555\n" \
  "s_mov_b32 exec_hi, 0x55555555\n" \
  "s_waitcnt vmcnt(0)\n" \
  "v_cmpx_ne_u64_e32 vcc, s[78:79], v[174:175]\n" \
  "s_cbranch_execz PBD_%=\n" \
  "PB_%=:\n" \
  "buffer_load_dwordx2 v[168:169], v100, %[rws], 0 offen offset:128 sc1\n" \
  "s_waitcnt vmcnt(0)\n" \
  "v_cmpx_ne_u64_e32 vcc, s[78:79], v[168:169]\n" \
  "s_cbranch_execz PBD_%=\n" \
  "s_sleep 2\n" \
  "s_sub_u32 s68, s68, 1\n" \
  "s_cbranch_scc0 PB_%=\n" \
  "s_endpgm\n" \
  "PBD_%=:\n" \
  "s_mov_b64 exec, -1\n" \
  "s_waitcnt lgkmcnt(0)\n" \
  "s_add_u32 m0, %[res], 0\n" \
  "v_and_or_b32 v160, v160, %[m32], v73\n" \
  "buffer_load_dwordx4 v160, %[rws], %[pe] offen sc1 lds\n" \
  "s_add_u32 m0, %[res], 1024\n" \
  "v_and_or_b32 v161, v161, %[m32], v73\n" \
  "buffer_load_dwordx4 v161, %[rws], %[pe] offen sc1 lds\n" \
  "s_waitcnt lgkmcnt(0)\n" \
  "s_barrier\n" \
  "ds_read2st64_b32 v[168:169], v89 offset1:2\n" \
  "s_add_u32 m0, %[res], 2048\n" \
  "v_and_or_b32 v162, v162, %[m32], v73\n" \
  "buffer_load_dwordx4 v162, %[rws], %[pe] offen sc1 lds\n" \
  "s_add_u32 m0, %[res], 3072\n" \
  "v_and_or_b32 v163, v163, %[m32], v73\n" \
  "buffer_load_dwordx4 v163, %[rws], %[pe] offen sc1 lds\n" \
  "v_bfi_b32 v144, %[mc0], v144, v120\n" \
  "v_bfi_b32 v145, %[mc0], v145, v121\n" \
  "v_bfi_b32 v146, %[mc0], v146, v122\n" \
  "v_bfi_b32 v147, %[mc0], v147, v123\n" \
  "v_add_u32_sdwa v140, v124, v140 dst_sel:DWORD dst_unused:UNUSED_PAD src0_sel:WORD_0 src1_sel:DWORD\n" \
  "v_bfi_b32 v148, %[mc0], v148, v124\n" \
  "v_add_u32_sdwa v141, v125, v141 dst_sel:DWORD dst_unused:UNUSED_PAD src0_sel:WORD_0 src1_sel:DWORD\n" \
  "v_bfi_b32 v149, %[mc0], v149, v125\n" \
  "s_cmp_lt_u32 %[tot], 385\n" \
  "s_cbranch_scc1 FM_%=\n" \
  "v_add_u32_sdwa v142, v126, v142 dst_sel:DWORD dst_unused:UNUSED_PAD src0_sel:WORD_0 src1_sel:DWORD\n" \
  "v_bfi_b32 v150, %[mc0], v150, v126\n" \
  "s_cmp_lt_u32 %[tot], 449\n" \
  "s_cbranch_scc1 FM_%=\n" \
  "v_add_u32_sdwa v143, v127, v143 dst_sel:DWORD dst_unused:UNUSED_PAD src0_sel:WORD_0 src1_sel:DWORD\n" \
  "v_bfi_b32 v151, %[mc0], v151, v127\n" \
  "FM_%=:\n" \
  "s_waitcnt lgkmcnt(0)\n" \
  "v_add_u32_e32 v169, v168, v169\n" \
  "v_mov_b32_e32 v170, v169\n" \
  "s_nop 1\n" \
  "v_permlane16_swap_b32_e32 v169, v170\n" \
  "v_add_u32_e32 v170, v169, v170\n" \
  "v_mov_b32_e32 v171, v170\n" \
  "v_and_b32_e32 v169, v92, v169\n" \
  "s_nop 0\n" \
  "v_permlane32_swap_b32_e32 v170, v171\n" \
  "v_add3_u32 v171, v170, v171, v91\n" \
  "v_and_b32_e32 v170, v93, v170\n" \
  "v_ashrrev_i32_e32 v171, 9, v171\n" \
  "v_add_u32_e32 v169, v169, v170\n" \
  "v_add_u32_e32 v168, v169, v168\n" \
  "ds_write2st64_b32 v89, v169, v168 offset1:2\n" \
  "v_cmp_lt_i32_e64 s[60:61], 0, v171\n" \
  "v_cmp_lt_i32_e64 s[62:63], 4, v171\n" \
  "v_cmp_lt_i32_e64 s[64:65], 8, v171\n" \
  "v_cmp_lt_i32_e64 s[66:67], 12, v171\n" \
  "v_mbcnt_lo_u32_b32 v172, s60, 0\n" \
  "s_bcnt1_i32_b64 s68, s[60:61]\n" \
  "v_mbcnt_hi_u32_b32 v172, s61, v172\n" \
  "v_add_lshl_u32 v172, v172, %[ngr], 3\n" \
  "s_add_u32 %[ngr], %[ngr], s68\n" \
  "v_mbcnt_lo_u32_b32 v173, s62, 0\n" \
  "s_bcnt1_i32_b64 s68, s[62:63]\n" \
  "v_mbcnt_hi_u32_b32 v173, s63, v173\n" \
  "v_add_lshl_u32 v173, v173, %[ngr], 3\n" \
  "s_add_u32 %[ngr], %[ngr], s68\n" \
  "ds_write2st64_b32 v90, v172, v173 offset0:0 offset1:8\n" \
  "v_mbcnt_lo_u32_b32 v174, s64, 0\n" \
  "s_bcnt1_i32_b64 s68, s[64:65]\n" \
  "v_mbcnt_hi_u32_b32 v174, s65, v174\n" \
  "v_add_lshl_u32 v174, v174, %[ngr], 3\n" \
  "s_add_u32 %[ngr], %[ngr], s68\n" \
  "v_mbcnt_lo_u32_b32 v175, s66, 0\n" \
  "s_bcnt1_i32_b64 s68, s[66:67]\n" \
  "v_mbcnt_hi_u32_b32 v175, s67, v175\n" \
  "v_add_lshl_u32 v175, v175, %[ngr], 3\n" \
  "s_add_u32 %[ngr], %[ngr], s68\n" \
  "ds_write2st64_b32 v90, v174, v175 offset0:16 offset1:24\n" \
  "v_cmp_lt_i32_e32 vcc, 16, v171\n" \
  "s_cbranch_vccnz BIG_%=\n" \
  "BGD_%=:\n" \
  "s_cmp_gt_u32 %[ngr], %[gmax]\n" \
  "s_cbranch_scc1 IR2_%=\n" \
  "TBD_%=:\n" \
  "s_cmp_lt_u32 %[tp], 129\n" \
  "s_cbranch_scc1 RC_%=\n" \
  "s_add_u32 m0, %[res], 4096\n" \
  "v_and_or_b32 v164, v164, %[m32], v73\n" \
  "buffer_load_dwordx4 v164, %[rws], %[pe] offen sc1 lds\n" \
  "s_cmp_lt_u32 %[tp], 161\n" \
  "s_cbranch_scc1 RC_%=\n" \
  "s_add_u32 m0, %[res], 5120\n" \
  "v_and_or_b32 v165, v165, %[m32], v73\n" \
  "buffer_load_dwordx4 v165, %[rws], %[pe] offen sc1 lds\n" \
  "s_cmp_lt_u32 %[tp], 193\n" \
  "s_cbranch_scc1 RC_%=\n" \
  "s_add_u32 m0, %[res], 6144\n" \
  "v_and_or_b32 v166, v166, %[m32], v73\n" \
  "buffer_load_dwordx4 v166, %[rws], %[pe] offen sc1 lds\n" \
  "s_cmp_lt_u32 %[tp], 225\n" \
  "s_cbranch_scc1 RC_%=\n" \
  "s_add_u32 m0, %[res], 7168\n" \
  "v_and_or_b32 v167, v167, %[m32], v73\n" \
  "buffer_load_dwordx4 v167, %[rws], %[pe] offen sc1 lds\n" \
  "RC_%=:\n" \
  "s_waitcnt lgkmcnt(0)\n" \
  "s_barrier\n" \
  "v_mov_b32_e32 v170, 71680\n" \
  "ds_read_b32 v170, v170\n" \
  "ds_read_b32 v112, v152\n" \
  "ds_read_b32 v113, v153\n" \
  "ds_read_b32 v114, v154\n" \
  "ds_read_b32 v115, v155\n" \
  "ds_read_b32 v116, v156\n" \
  "ds_read_b32 v117, v157\n" \
  "s_cmp_lt_u32 %[tot], 385\n" \
  "s_cbranch_scc1 RK_%=\n" \
  "ds_read_b32 v118, v158\n" \
  "s_cmp_lt_u32 %[tot], 449\n" \
  "s_cbranch_scc1 RK_%=\n" \
  "ds_read_b32 v119, v159\n" \
  "RK_%=:\n" \
  "s_movk_i32 s69, 0x3e00\n" \
  "s_waitcnt lgkmcnt(0)\n" \
  "v_readfirstlane_b32 %[st], v170\n" \
  "v_add_u32_e32 v128, v128, v112\n" \
  "v_bfi_b32 v152, s69, v128, v152\n" \
  "ds_read_b32 v152, v152 offset:20480\n" \
  "v_bfe_u32 v128, v128, 7, 2\n" \
  "v_add_u32_e32 v129, v129, v113\n" \
  "v_bfi_b32 v153, s69, v129, v153\n" \
  "ds_read_b32 v153, v153 offset:20480\n" \
  "v_bfe_u32 v129, v129, 7, 2\n" \
  "v_add_u32_e32 v130, v130, v114\n" \
  "v_bfi_b32 v154, s69, v130, v154\n" \
  "ds_read_b32 v154, v154 offset:20480\n" \
  "v_bfe_u32 v130, v130, 7, 2\n" \
  "v_add_u32_e32 v131, v131, v115\n" \
  "v_bfi_b32 v155, s69, v131, v155\n" \
  "ds_read_b32 v155, v155 offset:20480\n" \
  "v_bfe_u32 v131, v131, 7, 2\n" \
  "v_add_u32_e32 v132, v132, v116\n" \
  "v_bfi_b32 v156, s69, v132, v156\n" \
  "ds_read_b32 v156, v156 offset:20480\n" \
  "v_bfe_u32 v132, v132, 7, 2\n" \
  "v_add_u32_e32 v133, v133, v117\n" \
  "v_bfi_b32 v157, s69, v133, v157\n" \
  "ds_read_b32 v157, v157 offset:20480\n" \
  "v_bfe_u32 v133, v133, 7, 2\n" \
  "s_cmp_lt_u32 %[tot], 385\n" \
  "s_cbranch_scc1 TB_%=\n" \
  "v_add_u32_e32 v134, v134, v118\n" \
  "v_bfi_b32 v158, s69, v134, v158\n" \
  "ds_read_b32 v158, v158 offset:20480\n" \
  "v_bfe_u32 v134, v134, 7, 2\n" \
  "s_cmp_lt_u32 %[tot], 449\n" \
  "s_cbranch_scc1 TB_%=\n" \
  "v_add_u32_e32 v135, v135, v119\n" \
  "v_bfi_b32 v159, s69, v135, v159\n" \
  "ds_read_b32 v159, v159 offset:20480\n" \
  "v_bfe_u32 v135, v135, 7, 2\n" \
  "TB_%=:\n" \
  "s_cmp_lg_u32 %[st], 0\n" \
  "s_waitcnt lgkmcnt(0)\n" \
  "s_cbranch_scc1 WR_%=\n" \
  "v_lshl_add_u32 v152, v128, 1, v152\n" \
  "v_lshlrev_b32_e32 v128, 1, v152\n" \
  "ds_write_b32 v128, v136 offset:51200\n" \
  "ds_write_b16_d16_hi v152, v144 offset:40960\n" \
  "v_lshl_add_u32 v153, v129, 1, v153\n" \
  "v_lshlrev_b32_e32 v129, 1, v153\n" \
  "ds_write_b32 v129, v137 offset:51200\n" \
  "ds_write_b16_d16_hi v153, v145 offset:40960\n" \
  "v_lshl_add_u32 v154, v130, 1, v154\n" \
  "v_lshlrev_b32_e32 v130, 1, v154\n" \
  "ds_write_b32 v130, v138 offset:51200\n" \
  "ds_write_b16_d16_hi v154, v146 offset:40960\n" \
  "v_lshl_add_u32 v155, v131, 1, v155\n" \
  "v_lshlrev_b32_e32 v131, 1, v155\n" \
  "ds_write_b32 v131, v139 offset:51200\n" \
  "ds_write_b16_d16_hi v155, v147 offset:40960\n" \
  "v_lshl_add_u32 v156, v132, 1, v156\n" \
  "v_lshlrev_b32_e32 v132, 1, v156\n" \
  "s_mov_b64 exec, s[70:71]\n" \
  "ds_write_b32 v132, v140 offset:51200\n" \
  "ds_write_b16_d16_hi v156, v148 offset:40960\n" \
  "v_lshl_add_u32 v157, v133, 1, v157\n" \
  "v_lshlrev_b32_e32 v133, 1, v157\n" \
  "s_mov_b64 exec, s[72:73]\n" \
  "ds_write_b32 v133, v141 offset:51200\n" \
  "ds_write_b16_d16_hi v157, v149 offset:40960\n" \
  "s_cmp_lt_u32 %[tot], 385\n" \
  "s_cbranch_scc1 WR_%=\n" \
  "v_lshl_add_u32 v158, v134, 1, v158\n" \
  "v_lshlrev_b32_e32 v134, 1, v158\n" \
  "s_mov_b64 exec, s[74:75]\n" \
  "ds_write_b32 v134, v142 offset:51200\n" \
  "ds_write_b16_d16_hi v158, v150 offset:40960\n" \
  "s_cmp_lt_u32 %[tot], 449\n" \
  "s_cbranch_scc1 WR_%=\n" \
  "v_lshl_add_u32 v159, v135, 1, v159\n" \
  "v_lshlrev_b32_e32 v135, 1, v159\n" \
  "s_mov_b64 exec, s[76:77]\n" \
  "ds_write_b32 v135, v143 offset:51200\n" \
  "ds_write_b16_d16_hi v159, v151 offset:40960\n" \
  "WR_%=:\n" \
  "s_mov_b64 exec, -1\n" \
  "s_waitcnt lgkmcnt(0)\n" \
  "s_barrier\n" \
  "ds_read_b32 v168, v77\n" \
  "ds_read_b64 v[170:171], v76\n" \
  "s_add_u32 m0, %[stg], 4096\n" \
  "s_movk_i32 s69, 128\n" \
  "s_waitcnt lgkmcnt(0)\n" \
  "v_and_or_b32 v168, v168, %[m32], v73\n" \
  "buffer_load_dwordx4 v168, %[re], %[np32] offen lds\n" \
  "s_mov_b32 m0, %[stg]\n" \
  "v_mad_u32_u16 v172, v170, s69, v182\n" \
  "buffer_load_dwordx4 v172, %[rx], 0 offen sc1 lds\n" \
  "v_mad_u32_u16 v173, v170, s69, v183 op_sel:[1,0,0,0]\n" \
  "buffer_load_dwordx4 v173, %[rx], 0 offen offset:1024 sc1 lds\n" \
  "v_mad_u32_u16 v174, v171, s69, v184\n" \
  "buffer_load_dwordx4 v174, %[rx], 0 offen offset:2048 sc1 lds\n" \
  "v_mad_u32_u16 v175, v171, s69, v185 op_sel:[1,0,0,0]\n" \
  "buffer_load_dwordx4 v175, %[rx], 0 offen offset:3072 sc1 lds\n" \
  "s_waitcnt vmcnt(5)\n" \
  "s_barrier\n" \
  "s_branch FIN_%=\n" \
  "IRR_%=:\n" \
  "v_mov_b32_e32 v168, 71680\n" \
  "ds_write_b32 v168, v180\n" \
  "s_mov_b32 %[tot], 0\n" \
  "s_mov_b32 %[tp], 0\n" \
  "s_branch REG_%=\n" \
  "BIG_%=:\n" \
  "v_cmp_lt_i32_e64 s[60:61], 16, v171\n" \
  "v_cmp_lt_i32_e64 s[62:63], 20, v171\n" \
  "v_cmp_lt_i32_e64 s[64:65], 24, v171\n" \
  "v_cmp_lt_i32_e64 s[66:67], 28, v171\n" \
  "v_mbcnt_lo_u32_b32 v172, s60, 0\n" \
  "s_bcnt1_i32_b64 s68, s[60:61]\n" \
  "v_mbcnt_hi_u32_b32 v172, s61, v172\n" \
  "v_add_lshl_u32 v172, v172, %[ngr], 3\n" \
  "s_add_u32 %[ngr], %[ngr], s68\n" \
  "v_mbcnt_lo_u32_b32 v173, s62, 0\n" \
  "s_bcnt1_i32_b64 s68, s[62:63]\n" \
  "v_mbcnt_hi_u32_b32 v173, s63, v173\n" \
  "v_add_lshl_u32 v173, v173, %[ngr], 3\n" \
  "s_add_u32 %[ngr], %[ngr], s68\n" \
  "ds_write2st64_b32 v90, v172, v173 offset0:32 offset1:40\n" \
  "v_mbcnt_lo_u32_b32 v174, s64, 0\n" \
  "s_bcnt1_i32_b64 s68, s[64:65]\n" \
  "v_mbcnt_hi_u32_b32 v174, s65, v174\n" \
  "v_add_lshl_u32 v174, v174, %[ngr], 3\n" \
  "s_add_u32 %[ngr], %[ngr], s68\n" \
  "v_mbcnt_lo_u32_b32 v175, s66, 0\n" \
  "s_bcnt1_i32_b64 s68, s[66:67]\n" \
  "v_mbcnt_hi_u32_b32 v175, s67, v175\n" \
  "v_add_lshl_u32 v175, v175, %[ngr], 3\n" \
  "s_add_u32 %[ngr], %[ngr], s68\n" \
  "ds_write2st64_b32 v90, v174, v175 offset0:48 offset1:56\n" \
  "v_cmp_lt_i32_e32 vcc, 32, v171\n" \
  "s_cbranch_vccz BGD_%=\n" \
  "IR2_%=:\n" \
  "v_mov_b32_e32 v168, 71680\n" \
  "ds_write_b32 v168, v180\n" \
  "s_branch TBD_%=\n" \
  "FIN_%=:\n"
#define FRONT_CLOB "v3", "v11", "v18", "v19", "v20", "v21", "v22", "v23", "v48", "v49", "v50", "v51", "v52", "v53", "v54", "v55", "v56", "v57", "v58", "v59", "v60", "v61", "v62", "v63", "v112", "v113", "v114", "v115", "v116", "v117", "v118", "v119", "v120", "v121", "v122", "v123", "v124", "v125", "v126", "v127", "v128", "v129", "v130", "v131", "v132", "v133", "v134", "v135", "v136", "v137", "v138", "v139", "v140", "v141", "v142", "v143", "v144", "v145", "v146", "v147", "v148", "v149", "v150", "v151", "v152", "v153", "v154", "v155", "v156", "v157", "v158", "v159", "v160", "v161", "v162", "v163", "v164", "v165", "v166", "v167", "v168", "v169", "v170", "v171", "v172", "v173", "v174", "v175", "v176", "v177", "v178", "v179", "v180", "v181", "v186", "v187", "v188", "v189", "v190", "v191", "v192", "v193", "v194", "v195", "v196", "v197", "v198", "v199", "v200", "v201", "v202", "v203", "v204", "v205", "v206", "v207", "v208", "v209", "v210", "v211", "v212", "v213", "v214", "v215", "v216", "v217", "v218", "v219", "v220", "v221", "v249", "v250", "v251", "v252", "v253", "v254", "s60", "s61", "s62", "s63", "s64", "s65", "s66", "s67", "s68", "s69", "s70", "s71", "s72", "s73", "s74", "s75", "s76", "s77"
#define FRONT_E0 1

constexpr int LTILES = 20;
constexpr int STW = 5120;
constexpr int K2_ST = 0;
constexpr int K2_LS = K2_ST + 8 * STW;
constexpr int K2_LA = K2_LS + 8 * LTILES * 64;
constexpr int K2_FLAG = K2_LA + 8 * LTILES * 128;
constexpr int K2_LUT = K2_FLAG + 128;
constexpr int K2_WU = K2_LUT + 16 * LUTS;
constexpr int K2_RES = K2_WU + 16384;
constexpr int K1_WM = K2_RES, K1_E = K1_WM + 18432, K1_END = K1_E + NP * 32;
constexpr int K2_LDS = K2_RES + 8 * CW * 32 > K1_END ? K2_RES + 8 * CW * 32 : K1_END;
constexpr int K2_CNT = 0;
constexpr int K2_MK = K2_CNT + 4096;
constexpr int K2_TB = K2_MK + 16384;
constexpr int K1_OWN = K2_TB, K1_HIST = K1_OWN + 16384, K1_SID = K2_LS, K1_INV = K1_SID + 12544;
static_assert(K2_CNT == 0 && K2_LDS <= 163840 && K1_HIST + 4096 <= K2_LS && K1_INV + NP * 4 + 16 <= K2_FLAG && PA == 3 && CW == 256 && K2_TB == 20480 && K1_HIST == 36864 && K2_LS == 40960 &&
              K2_LA == 51200 && K2_FLAG == 71680 && K2_LUT == 71808 && K2_WU == 74112 && K2_RES == 90496 && K2_LUT % 32 == 0 && K2_RES % 128 == 0 &&
              CW % 32 == 0 && K2_LA < 65536, "lds");

DI bf16x8 asb(u32x4 v) { return __builtin_bit_cast(bf16x8, v); }
DI u32x4 pack8(const f32x16& x, int s, float sc) {
    u32x4 o;
#pragma unroll
    for (int j = 0; j < 4; j++) o[j] = pk2(x[8 * s + 2 * j] * sc, x[8 * s + 2 * j + 1] * sc);
    return o;
}
DI u32x8 cat2(u32x4 a, u32x4 b) { return __builtin_shufflevector(a, b, 0, 1, 2, 3, 4, 5, 6, 7); }
DI u32x16 cat4(u32x4 a, u32x4 b, u32x4 c, u32x4 d) { return __builtin_shufflevector(cat2(a, b), cat2(c, d), 0, 1, 2, 3, 4, 5, 6, 7, 8, 9, 10, 11, 12, 13, 14, 15); }
template <int I> DI bf16x8 sub4(u32x16 v) { return asb(__builtin_shufflevector(v, v, 4 * I, 4 * I + 1, 4 * I + 2, 4 * I + 3)); }
template <int I> DI bf16x8 sub4(u32x8 v) { return asb(__builtin_shufflevector(v, v, 4 * I, 4 * I + 1, 4 * I + 2, 4 * I + 3)); }
DI unsigned wave_max_scan(unsigned v) {
    v = max(v, dpp0<0x111>(v)); v = max(v, dpp0<0x112>(v)); v = max(v, dpp0<0x114>(v)); v = max(v, dpp0<0x118>(v));
    v = max(v, dppm<0x142, 0xa>(v)); v = max(v, dppm<0x143, 0xc>(v));
    return v;
}
typedef __attribute__((ext_vector_type(3))) unsigned u32x3;
typedef __attribute__((ext_vector_type(4))) short s16x4;

#define PIN8(a) asm volatile("" : "+v"(a[0]), "+v"(a[1]), "+v"(a[2]), "+v"(a[3]), "+v"(a[4]), "+v"(a[5]), "+v"(a[6]), "+v"(a[7]))
#define SB __builtin_amdgcn_sched_barrier(0)
DI bf16x8 trf(unsigned a0, unsigned a1) {
    const s16x4 lo = __builtin_amdgcn_ds_read_tr16_b64_v4i16((LDS s16x4*)(size_t)a0), hi = __builtin_amdgcn_ds_read_tr16_b64_v4i16((LDS s16x4*)(size_t)a1);
    return __builtin_shufflevector(lo, hi, 0, 1, 2, 3, 4, 5, 6, 7);
}
DI const char* mkptr(unsigned lo, unsigned hi) { return (const char*)(((uint64_t)hi << 32) | lo); }

__global__ __launch_bounds__(512, 2) void kf(const char* x_, const char* eattr_, const int* src_, const int* dst_, const char* wm_, const char* wu_, char* ws_, char* out_) {
    __shared__ __attribute__((aligned(128))) char lds[K2_LDS];
    u32x16 ka;
    asm volatile("s_load_dwordx16 %0, %1, 0" : "=&s"(ka) : "s"(__builtin_amdgcn_kernarg_segment_ptr()));
    const unsigned L = ldsaddr(lds);
    const unsigned tid = threadIdx.x, lane = tid & 63;
    const int wv = __builtin_amdgcn_readfirstlane(tid >> 6);
    const int b = (blockIdx.x & 7) * 32 + (blockIdx.x >> 3);
    auto clear = [&]() {
        const unsigned t_ = opq(tid);
        ldsw<u32x2>(L + K2_CNT + t_ * 8, u32x2{0, 0});
        ldsw<u32x4>(L + K2_MK + t_ * 32, u32x4{0, 0, 0, 0});
        ldsw<u32x4>(L + K2_MK + t_ * 32 + 16, u32x4{0, 0, 0, 0});
        ldsw<u32x2>(L + K2_FLAG, u32x2{0, 0});
    };
    clear();
    ldsw<u32x2>(L + K1_HIST + tid * 8, u32x2{0, 0});
    if (tid < LUTS) ldsw<u32x4>(L + K2_LUT + tid * 16, u32x4{0, 0, 0, 0});
    const unsigned stg = L + K2_ST + wv * STW, lba = L + K2_LA + wv * (LTILES * 128), lbs = L + K2_LS + wv * (LTILES * 64), res = L + K2_RES + wv * (CW * 32);
    const unsigned mkb = L + K2_MK + wv * 2048, own = L + K1_OWN + wv * 2048;
    unsigned op = tid * (PA * 4), orr = NP * 4 + tid * ((7 - PA) * 4), l16 = lane * 16;
    asm volatile("s_waitcnt lgkmcnt(0)" : "+s"(ka), "+v"(op), "+v"(orr), "+v"(l16)::"memory");
    const char *x = mkptr(ka[0], ka[1]), *eattr = mkptr(ka[2], ka[3]);
    char *ws = (char*)mkptr(ka[12], ka[13]), *out = (char*)mkptr(ka[14], ka[15]);
    const u32x4 rws = mkrsrc(ws, WS_TS);
    auto tlbp = [&]() {
        const unsigned o = lane << TLBS;
        asm volatile("s_mov_b32 m0, %0\n\ts_nop 0\n\tbuffer_load_dword %1, %2, 0 offen lds\n\tbuffer_load_dword %1, %3, 0 offen lds\n\t"
                     "buffer_load_dword %1, %4, 0 offen lds\n\tbuffer_load_dword %1, %5, 0 offen lds\n\tbuffer_load_dword %1, %6, 0 offen lds\n\tbuffer_load_dword %1, %7, 0 offen lds"
                     ::"s"(L + K2_LDS - 256), "v"(o), "s"(rws), "s"(mkrsrc(eattr, EE * 32)), "s"(mkrsrc(x, NN * 128)), "s"(mkrsrc(out, NN * 128)),
                       "s"(mkrsrc(mkptr(ka[8], ka[9]), 18432)), "s"(mkrsrc(mkptr(ka[10], ka[11]), 16384)) : "memory");
    };
    u32x3 dp, sp;
    u32x4 dr, sr_;
    u32x2 ep;
    asm volatile("s_cmp_eq_u32 %10, 7\n\ts_cbranch_scc1 1f\n\t"
                 "buffer_load_dwordx3 %0, %5, %7, 0 offen nt\n\tbuffer_load_dwordx4 %1, %6, %7, 0 offen nt\n\t"
                 "buffer_load_dwordx3 %2, %5, %8, 0 offen nt\n\tbuffer_load_dwordx4 %3, %6, %8, 0 offen nt\n"
                 "1:\n\tbuffer_load_dwordx2 %4, off, %9, 0 sc1\n\ts_barrier"
                 : "=&{v[0:2]}"(dp), "=&{v[4:7]}"(dr), "=&{v[8:10]}"(sp), "=&{v[12:15]}"(sr_), "=&{v[16:17]}"(ep)
                 : "v"(op), "v"(orr), "s"(mkrsrc(mkptr(ka[6], ka[7]) + b * (CH * 4), CH * 4)), "s"(mkrsrc(mkptr(ka[4], ka[5]) + b * (CH * 4), CH * 4)), "s"(rws), "s"(wv)
                 : "memory", "scc");
    const u32x4 re = mkrsrc(eattr, EE * 32);
    if (wv == 7) asm volatile("s_mov_b32 m0, %0\n\ts_nop 0\n\tbuffer_load_dword %1, %2, %3 offen lds" ::"s"(L + K2_LDS - 256), "v"(lane * 4), "s"(rws), "s"((int)WS_FLAG + b * FST) : "memory");
    const unsigned eo = b * (CH * 32) + wv * 1024;
#pragma unroll
    for (int i = 0; i < FRONT_E0; i++)
        asm volatile("s_mov_b32 m0, %0\n\ts_nop 0\n\tbuffer_load_dwordx4 %1, %2, %3 offen " ENTS "lds" ::"s"(L + K1_E + i * 8192 + wv * 1024), "v"(l16), "s"(re), "s"(eo + i * 8192) : "memory");
    if (tid < 32) {
        const unsigned row = tid >> 1, pr = (row & 3) | ((row & 4) << 1) | ((row & 8) >> 1);
        ldsw<unsigned short>(L + K2_LUT + row * LUTS + ((tid & 1) ? 96 + pr * 2 : 32 + row * 2), 0x3F80);
    }
    auto marks = [&](unsigned t_) -> u32x2 {
        const unsigned c = t_ >> 1, m = (((c & 31) * IDS) << 13) | ((t_ & 1) << 11) | c;
        return u32x2{m, m | (0x80000200u + ((c * 6) & 0x600))};
    };
    unsigned h16, erd, luta, lutt, low, pj0, pv0, pe0, pa0, wb, pada;
    u32x4 xr4, xc4;
    u32x8 w3, qf, idf;
    f32x16 y;
    u32x2 ow;
    unsigned ngr0 = wv * (LTILES * 8), irregular;
    const u32x4 rx = mkrsrc(ws + WS_P - (XB + 24) * 128, (NN + XB + 24) * 128);
    const unsigned u0 = (b * 8 + wv) * 16;
    {
        const unsigned r = lane & 31, h = lane >> 5, n2 = lane & 15;
        const unsigned xc0 = 16 * ((lane & 7) ^ (lane >> 5));
        xc4 = u32x4{xc0 + 3072, (xc0 ^ 32) + 2048, (xc0 ^ 64) + 1024, xc0 ^ 96};
        low = (lane & 1) * 16; pe0 = lbs + (lane & 56); pa0 = lba + (lane >> 1) * 4;
        const unsigned xrd = stg + ((r & 3) * 8 + (r >> 2)) * 128 + 16 * (h ^ ((r & 3) * 2 + (r >> 4)));
        xr4 = u32x4{xrd, xrd ^ 32, xrd ^ 64, xrd ^ 96};
        h16 = 16 * h; erd = stg + 4096 + r * 32 + 16 * h; luta = L + K2_LUT + 96 + 16 * h; lutt = L + K2_LUT + ((lane >> 4) & 1) * 32 + (lane & 3) * 8;
        pj0 = lba + r * 4; pv0 = lba + (lane & 12) * 8 + 16 * h;
        const unsigned tc = ((lane >> 2) & 3) * 128 + (lane & 8) * 8 + (lane & 16) * 2 + (lane & 3) * 8, tm = L + K2_WU + h * 1024 + tc;
        wb = L + K2_WU + 8192 + h * 512 + tc;
#pragma unroll
        for (int i = 0; i < 8; i++) idf[i] = (((r >> 2) & 1) == h && (unsigned)i == ((r >> 3) * 2 + ((r >> 1) & 1))) ? 0x3F80u << (16 * (r & 1)) : 0u;
        const unsigned cn = (wv & 1) * 256 + (wv >> 1) * 4 + n2 * 16, alt = (lane * 32 + res) << 14;
        const unsigned d0 = node_of(u0 + (lane >> 3)) * 128, d1 = node_of(u0 + 8 + (lane >> 3)) * 128;
        const unsigned pc = __builtin_ctzll(__ballot(d0 >= 0x40000000u)) >> 3;
        pada = ((L + K2_LUT) << 14) | pc;
        const unsigned a1 = own + n2 * 128 + 16 * (h ^ (n2 >> 1)), zr = own + pc * 128, xo = 16 * ((lane & 7) ^ (lane >> 4));
        const int sh = -b & 3;
        const u32x2 km = marks(tid);
        const u32x4 k0 = {lane, L + K2_MK + tid * 32, mkb + lane * 4, (lane >> 1) * 4};
        const u32x8 k1 = {l16, L + K2_CNT + (lane >> 4) * 1024 + cn, L + K2_TB + (lane >> 4) * 512 + cn, 384 - (lane >> 4) * 512,
                          (lane & 16) ? ~0u : 0u, (lane & 32) ? ~0u : 0u, lba + l16, lbs + l16};
        u32x16 k2 = {alt, alt + (2048 << 14), alt + (4096 << 14), alt + (6144 << 14),
                     (unsigned)WS_FLAG + (tid >> 1) * FST, (unsigned)WS_CELL + (tid >> 1) * CROW + b * 16 + (tid & 1) * 8,
                     tid * 4, tid * 128, L + K1_E + tid * (PA * 32), tid * 16, L + K1_INV + sh * 4 + (tid >> 1) * 4, (tid & 1) ? 1u : 0x10001u,
                     L + K2_FLAG + 32 + (lane & 7) * 4, (int)(lane & 7) < wv ? ~0u : 0u, tid * 8, 16384 + ((tid * 4) ^ (tid & 64))};
        const u32x8 k3 = {d0 + 1024 + xo, d1 + (xo ^ 64), l16 ^ ((lane & 16) << 2), r < 16 ? a1 : zr, r < 16 ? zr : a1, tm, tm ^ 64,
                          own + n2 * 128 + 16 * (((lane >> 4 & 1) * 2 + h) ^ (n2 & 7))};
        const u32x3 k4 = {own + (lane >> 3) * 128 + 16 * ((lane & 7) ^ (lane >> 3)), (unsigned)WS_P + d0 + (lane & 7) * 16, (unsigned)WS_P + d1 + (lane & 7) * 16};
        unsigned tot, tp;
        uint64_t tag;
#define TSF
        asm volatile("" : "+{v65}"(h16), "+{v[66:69]}"(xr4), "+{v70}"(erd), "+{v71}"(luta), "+{v72}"(lutt), "+{v74}"(pj0), "+{v75}"(pv0), "+{v78}"(wb), "+{v[24:31]}"(idf));
        asm volatile(FRONT_ASM
                     : "={v[80:81]}"(ow), "={v[32:39]}"(w3), "={v[40:47]}"(qf), "={v[222:237]}"(y), "=&{s[78:79]}"(tag), "+{v[96:111]}"(k2),
                       "+{v[0:2]}"(dp), "+{v[4:7]}"(dr), "+{v[8:10]}"(sp), "+{v[12:15]}"(sr_), "+{v[16:17]}"(ep),
                       [tot] "=&s"(tot), [tp] "=&s"(tp), [st] "=&s"(irregular), [ngr] "+s"(ngr0) TSF
                     : "{v73}"(low), "{v76}"(pe0), "{v77}"(pa0), "{v[182:185]}"(xc4), "{v[82:83]}"(km), "{v[84:87]}"(k0), "{v[88:95]}"(k1),
                       "{v[238:245]}"(k3), "{v[246:248]}"(k4),
                       [rws] "s"(rws), [rx] "s"(rx), [re] "s"(re), [rm] "s"(mkrsrc(mkptr(ka[8], ka[9]), 18432)), [ru] "s"(mkrsrc(mkptr(ka[10], ka[11]), 16384)),
                       [rxx] "s"(mkrsrc(x - 1024, NN * 128 + 1024)), [wv1k] "s"(wv * 1024), [eo] "s"(eo), [fo] "s"((int)WS_FLAG + b * FST),
                       [co] "s"((int)WS_CELL + b * CROW), [io] "s"((int)WS_ID + b * (IDS * 4)), [po] "s"((int)WS_E + (b * CH + sh) * 32), [pe] "s"((int)WS_E),
                       [ib] "s"((int)WS_ID + wv * (32 * IDS * 4) - 2048), [sh] "s"(sh << 18), [np32] "s"(NP * 32), [nr4] "s"(NR), [ca] "s"(85899), [xb] "s"(XB),
                       [m80] "s"(0x80000000u), [mc0] "s"(0xc0000000u), [m32] "s"(~31u), [nl2e] "s"(-1.4426950408889634f), [pada] "s"(pada),
                       [stg] "s"(stg), [res] "s"(res), [mkb] "s"(mkb), [gmax] "s"(wv * (LTILES * 8) + (LTILES - 2) * 8)
                     : "memory", "scc", "vcc", FRONT_CLOB);
        if (blockIdx.x == 0 && tid == 0) asm volatile("buffer_store_dwordx2 %0, off, %1, 0 sc1" ::"v"(u32x2{(unsigned)tag, (unsigned)(tag >> 32)}), "s"(rws) : "memory");
        STAMP(1, 2);
    }
    auto request = [&](int t) __attribute__((always_inline)) {
        const u32x2 e4 = ldsr<u32x2>(pe0 + t * 64);
        dma16(re, stg + 4096, ((ldsr<unsigned>(pa0 + t * 128) & ~31u) | low) + NP * 32);
        dma16s(rx, stg, (e4[0] & 0xffff) * 128 + xc4[0]); dma16s(rx, stg + 1024, (e4[0] >> 16) * 128 + xc4[1] + 1024);
        dma16s(rx, stg + 2048, (e4[1] & 0xffff) * 128 + xc4[2] + 2048); dma16s(rx, stg + 3072, (e4[1] >> 16) * 128 + xc4[3] + 3072);
    };
    auto plain = [&](const int ntile) __attribute__((always_inline)) {
            f32x16 ag = {0};
#pragma unroll 1
            for (int t = 0; t < ntile; t++) {
                request(t);
                const unsigned er = ldsr<unsigned>(pj0 + t * 128);
                const bf16x8 st = asb(ldsr<u32x4>(luta + (er & 15) * LUTS));
                const unsigned sa = lutt + (ldsr<unsigned>(pv0 + t * 128) & 15) * LUTS;
                const s16x4 s0 = __builtin_amdgcn_ds_read_tr16_b64_v4i16((LDS s16x4*)(size_t)(sa + 32)), s1 = __builtin_amdgcn_ds_read_tr16_b64_v4i16((LDS s16x4*)(size_t)sa);
                WAITVM(0);
                const bf16x8 ef = asb(ldsr<u32x4>(max((er >> 16) * 4 + h16, erd)));
                const bf16x8 x0 = asb(ldsr<u32x4>(xr4[0])), x1 = asb(ldsr<u32x4>(xr4[1])), x2 = asb(ldsr<u32x4>(xr4[2])), x3 = asb(ldsr<u32x4>(xr4[3]));
                f32x16 z0 = {0}, z1 = {0};
                z0 = MFMA(st, sub4<0>(qf), z0); z0 = MFMA(ef, sub4<0>(w3), z0);
                z0 = MFMA(x0, sub4<0>(idf), z0); z0 = MFMA(x2, sub4<1>(idf), z0);
                z1 = MFMA(st, sub4<1>(qf), z1); z1 = MFMA(ef, sub4<1>(w3), z1);
                z1 = MFMA(x1, sub4<0>(idf), z1); z1 = MFMA(x3, sub4<1>(idf), z1);
                float m[8];
#pragma unroll
                for (int g = 0; g < 8; g++) {
                    m[g] = 0.f;
#pragma unroll
                    for (int i = 0; i < 4; i++) {
                        const float z = g < 4 ? z0[4 * g + i] : z1[4 * (g - 4) + i];
                        m[g] = __builtin_fmaf(z, __builtin_amdgcn_rcpf(1.0f + __builtin_amdgcn_exp2f(z)), m[g]);
                    }
                }
                ag = MFMA(asb(u32x4{pk2(m[0], m[1]), pk2(m[2], m[3]), pk2(m[4], m[5]), pk2(m[6], m[7])}), __builtin_shufflevector(s0, s1, 0, 1, 2, 3, 4, 5, 6, 7), ag);
                asm volatile("s_waitcnt lgkmcnt(0)" ::: "memory");
            }
#pragma unroll
            for (int s = 0; s < 2; s++) {
                u32x4 l0, l1, h0, h1;
#pragma unroll
                for (int j = 0; j < 4; j++) {
                    const unsigned p = pk2(ag[8 * s + 2 * j], ag[8 * s + 2 * j + 1]);
                    const auto u = __builtin_amdgcn_permlane16_swap(p, 0u, false, false), v = __builtin_amdgcn_permlane16_swap(0u, p, false, false);
                    l0[j] = u[0]; l1[j] = u[1]; h0[j] = v[0]; h1[j] = v[1];
                }
                y = MFMA(trf(wb + s * 2048, wb + s * 2048 + 1024), asb(l0), y); y = MFMA(trf(wb + 4096 + s * 2048, wb + 5120 + s * 2048), asb(l1), y);
                y = MFMA(trf((wb ^ 64) + s * 2048, (wb ^ 64) + 1024 + s * 2048), asb(h0), y); y = MFMA(trf((wb ^ 64) + 4096 + s * 2048, (wb ^ 64) + 5120 + s * 2048), asb(h1), y);
            }
    };
    {
        const int ntile = irregular ? 1 : max((int)(ngr0 - wv * (LTILES * 8) + 7) >> 3, 1);
        float vmin = 1.0f;
        int nt = __builtin_amdgcn_readfirstlane(ntile);
        unsigned pj = pj0, pv = pv0, pe4 = pe0, pea = pa0;
        f32x16 y2;
        const u32x4 rx2 = rx;
        const u32x4 re2 = re;
#define TSOUT
#define QOUT "+{v64}"(vmin), "+{v74}"(pj), "+{v75}"(pv), "+{v76}"(pe4), "+{v77}"(pea), [rem] "+s"(nt) TSOUT
#define QIN "{v65}"(h16), "{v[66:69]}"(xr4), "{v70}"(erd), "{v71}"(luta), "{v72}"(lutt), "{v73}"(low), "{v78}"(wb), "{v[182:185]}"(xc4), \
            [rx] "s"(rx2), [re] "s"(re2), [np32] "s"(NP * 32), [stg] "s"(stg), [luts] "s"(LUTS), [c128] "s"(128), [m32] "s"(~31u)
        asm volatile(QLOOP_ASM
                     : "={v[190:205]}"(y2), QOUT
                     : "{v[222:237]}"(y), "{v[24:31]}"(idf), "{v[32:39]}"(w3), "{v[40:47]}"(qf), QIN
                     : "memory", "scc", QLOOP_CLOB);
        STAMP(1, 3);
        if (__builtin_expect(__any(!(vmin >= 0x1p-100f) || !(__builtin_fabsf(y2[0]) < __builtin_inff())), 0)) {
            plain(ntile);
        } else y = y2;
    }
    if (__builtin_expect(irregular != 0, 0)) {
        const unsigned n0 = ow[1] & 0xffff, n1 = ow[1] >> 16, i0 = wave_incl_scan(n0), i1 = wave_incl_scan(n1);
        const unsigned totp = __builtin_amdgcn_readlane(i0, 63), total = __builtin_amdgcn_readlane(i1, 63) + totp;
        const unsigned sp0 = i0 - n0, sp1 = i1 - n1 + totp;
        const __amdgpu_buffer_rsrc_t rall = __builtin_amdgcn_make_buffer_rsrc(ws, 0, (int)WS_TS, 0x00020000);
        unsigned w0 = 0, win = 512;
        while (true) {
            WAITVM(0);
            lds_barrier();
            clear();
            lds_barrier();
            const unsigned t_ = opq(tid), lane = t_ & 63;
            const unsigned pl = totp - min(totp, w0);
            const int wn = min(total - w0, pl > CW ? min(win, (unsigned)CW) : win), wp = min(pl, (unsigned)wn);
            const u32x2 mv = marks(t_);
            {
                const int s0 = sp0 - w0, s1 = sp1 - w0;
                if (max(s0, 0) < min(s0 + (int)n0, wn)) ldsw<unsigned>(mkb + max(s0, 0) * 4, mv[0] + (((ow[0] & 0xffff) - s0) << 13));
                if (max(s1, 0) < min(s1 + (int)n1, wn)) ldsw<unsigned>(mkb + max(s1, 0) * 4, mv[1] + (((ow[0] >> 16) - s1) << 13));
            }
            const unsigned pa = pada;
            for (int i = lane; i < LTILES * 32; i += 64) { ldsw<unsigned>(lba + i * 4, pa); ldsw<unsigned short>(lbs + i * 2, 0xffff); }
            if (w0 + wn < total) ldsw<unsigned>(L + K2_FLAG + 4, 1u);
            unsigned ca[8], wa[8], sr[8], carry = 0;
#pragma unroll
            for (int u = 0; u < 8; u++) {
                const unsigned m = max(wave_max_scan(ldsr<unsigned>(mkb + u * 256 + lane * 4)), carry);
                carry = __builtin_amdgcn_readlane(m, 63);
                const int slot = u * 64 + lane;
                const unsigned pos = wv * (32 * IDS) + ((m >> 13) & 0x1ffff) + slot - 512;
                ca[u] = wa[u] = sr[u] = 0;
                if (slot < wn) {
                    const unsigned id = __builtin_amdgcn_raw_buffer_load_b32(rall, (unsigned)WS_ID + pos * 4, 0, 16);
                    ca[u] = (m & 0xe00) | (__builtin_amdgcn_alignbit(id, id, 28) & 0x1fc);
                    __hip_atomic_fetch_add((LDS unsigned*)(size_t)(L + K2_CNT + ca[u]), 1u, __ATOMIC_RELAXED, __HIP_MEMORY_SCOPE_WORKGROUP);
                    const unsigned eo = (m & 255) * (CH * 32) + (id & 0xffff);
                    wa[u] = slot < wp ? ((res + slot * 32) << 14) | (id & 15) : eo;
                    sr[u] = ((id >> 16) & 0x3fff) | ((m << 3) & 0x4000);
                    if (slot < wp) {
                        ldsw<u32x4>(res + slot * 32, __builtin_amdgcn_raw_buffer_load_b128(rall, (unsigned)WS_E + (eo & ~31u), 0, 16));
                        ldsw<u32x4>(res + slot * 32 + 16, __builtin_amdgcn_raw_buffer_load_b128(rall, (unsigned)WS_E + (eo & ~31u) + 16, 0, 16));
                    }
                }
            }
            lds_barrier();
            unsigned ngr = 0;
            {
                const unsigned cn = (wv & 1) * 256 + (wv >> 1) * 4 + (lane & 15) * 16;
                unsigned sum = 0;
#pragma unroll
                for (int k = 0; k < 8; k++) { const unsigned c = ldsr<unsigned>(L + K2_CNT + k * 512 + cn); if (lane < 16) ldsw<unsigned>(L + K2_CNT + k * 512 + cn, sum); sum += c; }
                const int ng = (int)((sum + 3) >> 2) - (int)(lane >> 4);
                for (int j0 = 0; j0 < 32; j0 += 4) {
                    const unsigned long long m = __ballot(ng > j0);
                    ldsw<unsigned>(L + K2_TB + (j0 + (lane >> 4)) * 512 + cn, (__builtin_amdgcn_mbcnt_hi(m >> 32, __builtin_amdgcn_mbcnt_lo(m, 0)) + ngr + wv * (LTILES * 8)) * 4);
                    ngr += __popcll(m);
                }
                if (ng > 32 || ngr > (LTILES - 2) * 8) ldsw<unsigned>(L + K2_FLAG, 1u);
            }
            lds_barrier();
            const u32x2 flags = ldsr<u32x2>(L + K2_FLAG);
            if (__builtin_amdgcn_readfirstlane(flags[0])) { win = 16; continue; }
            const bool more = __builtin_amdgcn_readfirstlane(flags[1]);
#pragma unroll
            for (int u = 0; u < 8; u++)
                if ((int)(u * 64 + lane) < wn) {
                    const unsigned rk = __hip_atomic_fetch_add((LDS unsigned*)(size_t)(L + K2_CNT + ca[u]), 1u, __ATOMIC_RELAXED, __HIP_MEMORY_SCOPE_WORKGROUP);
                    const unsigned sl = ldsr<unsigned>(L + K2_TB + (rk >> 2) * 512 + (ca[u] & 0x1fc)) + (rk & 3);
                    ldsw<unsigned>(L + K2_LA + sl * 4, wa[u]); ldsw<unsigned short>(L + K2_LS + sl * 2, sr[u]);
                }
            __builtin_amdgcn_s_waitcnt(0x0F70);
            lds_barrier();
            const int ntile = (ngr + 7) >> 3;
            if (ntile > 0) plain(ntile);
            w0 += wn;
            if (!more) break;
        }
    }
    {
        const unsigned lane = opq(tid) & 63, nd = node_of(u0 + (lane & 15));
        float t[16];
#pragma unroll
        for (int i = 0; i < 16; i++) t[i] = __builtin_amdgcn_exp2f(y[i]);
        SB;
#pragma unroll
        for (int i = 0; i < 16; i++) t[i] = __builtin_fmaf(t[i], -1.4426950408889634f, -1.4426950408889634f);
        SB;
#pragma unroll
        for (int i = 0; i < 16; i++) t[i] = __builtin_amdgcn_rcpf(t[i]);
        SB;
        unsigned o[8];
#pragma unroll
        for (int i = 0; i < 8; i++) o[i] = pk2(y[2 * i] * t[2 * i], y[2 * i + 1] * t[2 * i + 1]);
        asm volatile("" : "+v"(o[0]), "+v"(o[1]), "+v"(o[2]), "+v"(o[3]), "+v"(o[4]), "+v"(o[5]), "+v"(o[6]), "+v"(o[7]));
        const __amdgpu_buffer_rsrc_t ro = __builtin_amdgcn_make_buffer_rsrc(out, 0, NN * 128, 0x00020000);
#pragma unroll
        for (int i = 0; i < 2; i++) {
            const auto a = __builtin_amdgcn_permlane32_swap(o[4 * i], o[4 * i + 2], false, false), c = __builtin_amdgcn_permlane32_swap(o[4 * i + 1], o[4 * i + 3], false, false);
            __builtin_amdgcn_raw_buffer_store_b128(u32x4{a[0], c[0], a[1], c[1]}, ro, nd * 128 + (lane & 16) * 4 + (lane & 32) / 2 + i * 32, 0, 0);
        }
    }
    STAMP(1, 4);
}

extern "C" void kernel_launch(void* const* d_in, const int* in_sizes, int n_in, void* d_out, int out_size, void* d_ws, size_t ws_size,
                              hipStream_t stream) {
    kf<<<256, 512, 0, stream>>>((const char*)d_in[0], (const char*)d_in[1], (const int*)d_in[2], (const int*)d_in[3], (const char*)d_in[4],
                                (const char*)d_in[5], (char*)d_ws, (char*)d_out);
}
```
